```python
import torch

def _softmax(x, axis=-1):
    return torch.softmax(x, dim=axis)
def _gelu(x):
    return torch.nn.functional.gelu(x, approximate="tanh")
def _silu(x):
    return torch.nn.functional.silu(x)
def _sigmoid(x):
    return torch.sigmoid(x)
def _layernorm(x, axis=-1, eps=1e-5):
    m = x.mean(dim=axis, keepdim=True)
    v = x.var(dim=axis, keepdim=True, unbiased=False)
    return (x - m) / torch.sqrt(v + eps)
def _scatter_add(tgt, idx, src, axis=0):
    return tgt.clone().index_add_(axis, idx, src)
def _segment_sum(src, seg, n):
    out = torch.zeros((n,) + src.shape[1:], dtype=src.dtype)
    return out.index_add_(0, seg, src)
def _pad(x, pw):
    flat = tuple(v for pair in reversed(pw) for v in pair)
    return torch.nn.functional.pad(x, flat)

kernel_name = 'concat_heads__ssm_conv_s5__softmax_full_mha__prompt'

def setup_inputs():

    _w = (lambda a: (lambda t: t.to(torch.bfloat16) if t.is_floating_point() else t)(torch.as_tensor(a)))
    def _A_setup():
        B, S, D = 1, 2048, 640
        P = D
        Lambda_re = -0.5 * (lambda _x: torch.exp(torch.as_tensor(_x)))((lambda *s: torch.randn(*s, dtype=torch.bfloat16) * 0.8)(P))
        Lambda_im = (lambda *s: torch.randn(*s, dtype=torch.bfloat16) * 0.8)(P) * 1.0
        B_re = (lambda *s: torch.randn(*s, dtype=torch.bfloat16) * 0.8)(P, D) * (D ** -0.5)
        B_im = (lambda *s: torch.randn(*s, dtype=torch.bfloat16) * 0.8)(P, D) * (D ** -0.5)
        C_re = (lambda *s: torch.randn(*s, dtype=torch.bfloat16) * 0.8)(D, P) * (P ** -0.5)
        C_im = (lambda *s: torch.randn(*s, dtype=torch.bfloat16) * 0.8)(D, P) * (P ** -0.5)
        log_dt = (lambda *s: torch.randn(*s, dtype=torch.bfloat16) * 0.8)(P) * 0.3 - 2.0
        Dskip = (lambda *s: torch.randn(*s, dtype=torch.bfloat16) * 0.8)(D)
        return {
            "x": _w((lambda *s: torch.randn(*s, dtype=torch.bfloat16) * 0.05)(B, S, D)),
            "Lambda_re": _w(Lambda_re),
            "Lambda_im": _w(Lambda_im),
            "B_re": _w(B_re),
            "B_im": _w(B_im),
            "C_re": _w(C_re),
            "C_im": _w(C_im),
            "log_dt": _w(log_dt),
            "Dskip": _w(Dskip),
        }
    _A_in = _A_setup()

    def _B_setup():
        B, S, D = 1, 2048, 640
        return {
            "x": _w((lambda *s: torch.randn(*s, dtype=torch.bfloat16) * 0.2)(B, S, D)),
            "wqkv": _w((lambda *s: torch.randn(*s, dtype=torch.bfloat16) * 0.2)(D, 3 * D)),
            "wo": _w((lambda *s: torch.randn(*s, dtype=torch.bfloat16) * 0.05)(D, D)),
        }
    _B_in = _B_setup()

    return {
        "A_x": _A_in["x"],
        "A_Lambda_re": _A_in["Lambda_re"],
        "A_Lambda_im": _A_in["Lambda_im"],
        "A_B_re": _A_in["B_re"],
        "A_B_im": _A_in["B_im"],
        "A_C_re": _A_in["C_re"],
        "A_C_im": _A_in["C_im"],
        "A_log_dt": _A_in["log_dt"],
        "A_Dskip": _A_in["Dskip"],
        "B_x": _B_in["x"],
        "B_wqkv": _B_in["wqkv"],
        "B_wo": _B_in["wo"],
    }

def reference(A_x, A_Lambda_re, A_Lambda_im, A_B_re, A_B_im, A_C_re, A_C_im, A_log_dt, A_Dskip, B_x, B_wqkv, B_wo):
    def _A_ref(x, Lambda_re, Lambda_im, B_re, B_im, C_re, C_im, log_dt, Dskip):
        Bsz, L, H = x.shape
        Lam = Lambda_re + 1j * Lambda_im
        Bmat = B_re + 1j * B_im
        Cmat = C_re + 1j * C_im
        dt = torch.exp(log_dt)
        Lam_bar = torch.exp(dt * Lam)
        B_bar = ((Lam_bar - 1.0) / Lam)[:, None] * Bmat
        Bu = torch.einsum("bld,pd->blp", x + 0j, B_bar)
        h = torch.zeros_like(Bu[:, 0, :])
        hs = []
        for t in range(L):
            h = Lam_bar[None, :] * h + Bu[:, t, :]
            hs.append(h)
        hseq = torch.stack(hs, 1)
        y = 2.0 * torch.einsum("blp,dp->bld", hseq, Cmat).real + x * Dskip[None, None, :]
        return y

    def _B_ref(x, wqkv, wo):
        B, S, D = x.shape
        H = 32; Dh = D // H
        qkv = (lambda x, *d: x.permute(*d))((x @ wqkv).reshape(B, S, 3, H, Dh), 2, 0, 3, 1, 4)
        q, k, v = qkv[0], qkv[1], qkv[2]
        scale = Dh ** -0.5
        scores = torch.einsum("bhqd,bhkd->bhqk", q, k) * scale
        p = _softmax(scores, axis=-1)
        o = torch.einsum("bhqk,bhkd->bhqd", p, v)
        return (lambda x, *d: x.permute(*d))(o, 0, 2, 1, 3).reshape(B, S, D) @ wo

    a = _A_ref(A_x, A_Lambda_re, A_Lambda_im, A_B_re, A_B_im, A_C_re, A_C_im, A_log_dt, A_Dskip)
    b = _B_ref(B_x, B_wqkv, B_wo)
    return torch.concatenate([a, b], axis=-1)
```

```cpp
#include <hip/hip_runtime.h>
#include <hip/hip_bf16.h>
#include <stdint.h>

typedef __attribute__((ext_vector_type(8))) __bf16 bf16x8;
typedef __attribute__((ext_vector_type(4))) __bf16 bf16x4;
typedef __attribute__((ext_vector_type(4))) short s16x4;
typedef __attribute__((ext_vector_type(16))) float f32x16;
typedef __attribute__((ext_vector_type(4))) float f32x4;
typedef __attribute__((ext_vector_type(2))) float f32x2;
typedef __attribute__((ext_vector_type(4))) unsigned u32x4;
typedef __attribute__((ext_vector_type(2))) unsigned u32x2;
typedef unsigned short u16;
#define LAS __attribute__((address_space(3)))
#define GAS __attribute__((address_space(1)))
typedef LAS char* ldsp;
typedef GAS unsigned long long gu64;
typedef GAS unsigned gu32;

constexpr int SEQ = 2048, DM = 640, NH = 32, DH = 20;
constexpr int KVREC = 2688;
constexpr int KOFF = 1344;
constexpr int QREC = 1280;

constexpr size_t WS_CTR = 0;
constexpr size_t WS_GRAN = 4096;
constexpr size_t WS_ZERO_BYTES = WS_GRAN + 16 * 640 * 2 * 8;
constexpr size_t WS_N2 = 262144;
constexpr size_t WS_KV = WS_N2 + 16384;
constexpr size_t WS_Q = WS_KV + (size_t)NH * 64 * KVREC;
constexpr size_t WS_O = WS_Q + (size_t)NH * 64 * QREC;
constexpr size_t WS_H = WS_O + (size_t)SEQ * DM * 2;
constexpr size_t WS_END = WS_H + (size_t)SEQ * 2 * DM * 2;

constexpr float QSCALE = 0.22360679774997896f * 1.4426950408889634f;
constexpr float SAFE_BOUND = 40.0f;

#define MFMA32(a, b, c) __builtin_amdgcn_mfma_f32_32x32x16_bf16(a, b, c, 0, 0, 0)
#define MFMA8(a, b, c) __builtin_amdgcn_mfma_f32_32x32x8bf16_1k(a, b, c, 0, 0, 0)

__device__ __forceinline__ void glds16(const void* sbase, unsigned voff, unsigned lds_addr) {
  unsigned keep;
  asm volatile("s_mov_b32 %0, m0\n\ts_mov_b32 m0, %3\n\ts_nop 0\n\tglobal_load_lds_dwordx4 %1, %2\n\ts_mov_b32 m0, %0"
               : "=&s"(keep) : "v"(voff), "s"(sbase), "s"(lds_addr) : "memory");
}
template <int N> __device__ __forceinline__ void wait_vm_bar() {
  asm volatile("s_waitcnt vmcnt(%0) lgkmcnt(0)\n\ts_barrier" ::"i"(N) : "memory");
}
__device__ __forceinline__ void bar_lds() { asm volatile("s_waitcnt lgkmcnt(0)\n\ts_barrier" ::: "memory"); }
__device__ __forceinline__ float bf2f(u16 v) { return __uint_as_float((unsigned)v << 16); }
__device__ __forceinline__ float bflo(unsigned v) { return __uint_as_float(v << 16); }
__device__ __forceinline__ float bfhi(unsigned v) { return __uint_as_float(v & 0xffff0000u); }
__device__ __forceinline__ unsigned pack2(float lo, float hi) {
  typedef __attribute__((ext_vector_type(2))) __bf16 bf16x2;
  bf16x2 r; r[0] = (__bf16)lo; r[1] = (__bf16)hi;
  return __builtin_bit_cast(unsigned, r);
}
__device__ __forceinline__ float rbf(float v) { return (float)(__bf16)v; }
__device__ __forceinline__ bf16x8 ldsA(ldsp p) { return *(const LAS bf16x8*)p; }
__device__ __forceinline__ s16x4 ldstr(ldsp p) {
  typedef short v4i16_t __attribute__((ext_vector_type(4)));
  return __builtin_bit_cast(s16x4, __builtin_amdgcn_ds_read_tr16_b64_v4i16((LAS v4i16_t*)p));
}

template <int BK> __device__ __forceinline__ int swz_nk(int row) { return BK == 64 ? ((row >> 1) & 7) : ((row >> 2) & 3); }

template <int BK, int NST, bool BKN, int NT, bool SWAP, int NK, class BBase>
__device__ __forceinline__ void gemm_core(f32x16 (&acc)[2][NT], ldsp smem, int wave, int lane, const char* abase,
                                          const unsigned (&aoff)[BK / 16], BBase bbase, const unsigned (&boff)[BK * NT / 32]) {
  constexpr int ASZ = 128 * BK * 2, BSZ = 64 * NT * BK * 2, STG = ASZ + BSZ;
  constexpr int NA = BK / 16, NB = BK * NT / 32, L = NA + NB, KS = BK / 16, RP = BK * 2;
  const unsigned lds0 = (unsigned)(uintptr_t)smem;
  const int wm = wave >> 1, wn = wave & 1, r = lane & 31, hh = lane >> 5;
  auto issue = [&](int stage, int buf) {
    const char* ab = abase + (size_t)stage * (BK * 2);
    const unsigned l0 = lds0 + buf * STG;
#pragma unroll
    for (int i = 0; i < NA; ++i) glds16(ab, aoff[i], l0 + (wave * NA + i) * 1024);
    const char* bb = bbase(stage);
#pragma unroll
    for (int i = 0; i < NB; ++i) glds16(bb, boff[i], l0 + ASZ + (wave * NB + i) * 1024);
  };
  unsigned fa[KS], fb[BKN ? NT : 1];
#pragma unroll
  for (int ks = 0; ks < KS; ++ks) fa[ks] = r * RP + (((2 * ks + hh) ^ swz_nk<BK>(r)) << 4);
  if constexpr (BKN) {
    const int nh = (lane >> 4) & 1, q = (lane & 15) >> 2, p = lane & 3;
#pragma unroll
    for (int nt = 0; nt < NT; ++nt)
      fb[nt] = ASZ + (8 * hh + q) * 256 + (((((2 * wn + nt) ^ q) << 2) + 2 * nh + (p >> 1)) << 4) + (p & 1) * 8;
  } else fb[0] = 0;
  bf16x8 fA[2][2], fB[2][NT];
  auto loadf = [&](int set, ldsp sb, int ks) {
#pragma unroll
    for (int mt = 0; mt < 2; ++mt) fA[set][mt] = ldsA(sb + fa[ks] + (wm * 64 + mt * 32) * RP);
#pragma unroll
    for (int nt = 0; nt < NT; ++nt) {
      if constexpr (BKN) {
        s16x4 lo = ldstr(sb + fb[nt] + ks * 16 * 256), hi = ldstr(sb + fb[nt] + (ks * 16 + 4) * 256);
        typedef __attribute__((ext_vector_type(8))) short s16x8;
        s16x8 v = {lo[0], lo[1], lo[2], lo[3], hi[0], hi[1], hi[2], hi[3]};
        fB[set][nt] = __builtin_bit_cast(bf16x8, v);
      } else {
        fB[set][nt] = ldsA(sb + ASZ + fa[ks] + (wn * 32 * NT + nt * 32) * RP);
      }
    }
  };
#pragma unroll
  for (int s = 0; s < NST; ++s) issue(s, s);
  wait_vm_bar<(NST - 1) * L>();
  loadf(0, smem, 0);
  int buf = 0;
  for (int kt = 0; kt < NK; ++kt) {
    ldsp sb = smem + buf * STG;
    const int nbuf = (buf + 1 == NST) ? 0 : buf + 1;
#pragma unroll
    for (int ks = 0; ks < KS; ++ks) {
      if (ks < KS - 1) {
        loadf((ks + 1) & 1, sb, ks + 1);
      } else if (kt + 1 < NK) {
        const int rem = NK - 2 - kt;
        if (rem >= NST - 2) wait_vm_bar<(NST - 2) * L>();
        else if (NST > 3 && rem == 1) wait_vm_bar<L>();
        else wait_vm_bar<0>();
        if (kt + NST < NK) issue(kt + NST, buf);
        loadf(0, smem + nbuf * STG, 0);
      }
#pragma unroll
      for (int mt = 0; mt < 2; ++mt)
#pragma unroll
        for (int nt = 0; nt < NT; ++nt)
          acc[mt][nt] = SWAP ? MFMA32(fB[ks & 1][nt], fA[ks & 1][mt], acc[mt][nt]) : MFMA32(fA[ks & 1][mt], fB[ks & 1][nt], acc[mt][nt]);
    }
    buf = nbuf;
  }
}

template <int BK, int N, class RowFn>
__device__ __forceinline__ void nk_offsets(unsigned (&off)[N], int wave, int lane, RowFn rowfn) {
  constexpr int CPR = BK / 8, RPI = 64 / CPR;
#pragma unroll
  for (int i = 0; i < N; ++i) {
    const int row = (wave * N + i) * RPI + lane / CPR, ph = lane % CPR;
    off[i] = rowfn(row) + ((ph ^ swz_nk<BK>(row)) << 4);
  }
}

constexpr int K1_LDS = 4 * 32768;
__global__ __launch_bounds__(256, 1) void k1_qkv(const u16* __restrict__ x, const u16* __restrict__ w, char* __restrict__ ws) {
  extern __shared__ __attribute__((aligned(16))) char smem_[];
  ldsp smem = (ldsp)smem_;
  const int tid = threadIdx.x, lane = tid & 63, wave = __builtin_amdgcn_readfirstlane(tid >> 6);
  const int b = blockIdx.x, xcd = b & 7, idx = b >> 3;
  const int rt = 4 * (xcd & 3) + (idx & 3), ct = 8 * (xcd >> 2) + (idx >> 2);
  if (tid < 41) *(u32x4*)(ws + (size_t)b * 656 + tid * 16) = (u32x4){0u, 0u, 0u, 0u};

  unsigned aoff[4], boff[4];
  nk_offsets<64, 4>(aoff, wave, lane, [&](int row) { return (unsigned)(row * (DM * 2)); });
#pragma unroll
  for (int i = 0; i < 4; ++i) {
    const int row = (wave * 4 + i) * 4 + (lane >> 4), ph = lane & 15;
    int c = ph ^ ((row & 3) << 2);
    if (c == 15) c = 14;
    boff[i] = row * (3 * DM * 2) + c * 16;
  }
  const char* abase = (const char*)x + (size_t)rt * 128 * (DM * 2);
  const char* bb0 = (const char*)w + ct * 240;
  f32x16 acc[2][2];
#pragma unroll
  for (int i = 0; i < 2; ++i)
#pragma unroll
    for (int j = 0; j < 2; ++j) acc[i][j] = (f32x16){};
  gemm_core<64, 4, true, 2, true, 10>(acc, smem, wave, lane, abase, aoff,
                                      [&](int s) { return bb0 + (size_t)s * (64 * 3 * DM * 2); }, boff);
  constexpr int CP = 264;
  bar_lds();
  {
    const int wm = wave >> 1, wn = wave & 1, r = lane & 31, hh = lane >> 5;
#pragma unroll
    for (int mt = 0; mt < 2; ++mt)
#pragma unroll
      for (int nt = 0; nt < 2; ++nt)
#pragma unroll
        for (int g = 0; g < 4; ++g) {
          u32x2 v = {pack2(acc[mt][nt][4 * g], acc[mt][nt][4 * g + 1]), pack2(acc[mt][nt][4 * g + 2], acc[mt][nt][4 * g + 3])};
          *(LAS u32x2*)(smem + (wm * 64 + mt * 32 + r) * CP + (wn * 64 + nt * 32 + 8 * g + 4 * hh) * 2) = v;
        }
  }
  bar_lds();
  float* n2 = (float*)(ws + WS_N2);
#pragma unroll
  for (int i = 0; i < 3; ++i) {
    const int row = tid & 127, part = (wave >> 1) + 2 * i, gp = 6 * ct + part, type = gp >> 5, head = gp & 31;
    if (type < 2) {
      unsigned v[10];
#pragma unroll
      for (int j = 0; j < 5; ++j) {
        u32x2 t = *(const LAS u32x2*)(smem + row * CP + part * 40 + j * 8);
        v[2 * j] = t[0]; v[2 * j + 1] = t[1];
      }
      if (type == 0) {
#pragma unroll
        for (int j = 0; j < 10; ++j) v[j] = pack2(bflo(v[j]) * QSCALE, bfhi(v[j]) * QSCALE);
      }
      float ss = 0.f;
#pragma unroll
      for (int j = 0; j < 10; ++j) { const float a = bflo(v[j]), c = bfhi(v[j]); ss = fmaf(a, a, ss); ss = fmaf(c, c, ss); }
#pragma unroll
      for (int o = 32; o >= 1; o >>= 1) ss = fmaxf(ss, __shfl_xor(ss, o));
      const int grow = rt * 128 + row, t32 = grow >> 5, rr = grow & 31;
      char* rec = type == 0 ? ws + WS_Q + ((size_t)head * 64 + t32) * QREC : ws + WS_KV + ((size_t)head * 64 + t32) * KVREC + KOFF;
      *(u32x4*)(rec + rr * 16) = (u32x4){v[0], v[1], v[2], v[3]};
      *(u32x4*)(rec + 512 + rr * 16) = (u32x4){v[4], v[5], v[6], v[7]};
      *(u32x2*)(rec + 1024 + rr * 8) = (u32x2){v[8], v[9]};
      if (lane == 0) n2[(type * 32 + head) * 32 + (grow >> 6)] = ss;
    }
  }
  if (6 * ct + 5 >= 64) {
    const int q = (lane & 15) >> 2, p = lane & 3, i16 = lane & 15, grp = tid >> 4;
#pragma unroll
    for (int it = 0; it < 8; ++it) {
      const int item = it * 16 + grp;
      const int cg = item & 7, kt4 = (item >> 3) & 3, sh = item >> 5, s = sh >> 1, h2 = sh & 1;
      const int row0 = 32 * kt4 + 16 * s + 4 * h2 + q;
      ldsp a = smem + row0 * CP + (16 * cg + 4 * p) * 2;
      const s16x4 lo = ldstr(a), hi = ldstr(a + 8 * CP);
      const int c = 16 * cg + i16, part = c / 20, d = c - part * 20, gp = 6 * ct + part;
      if (c < 120 && gp >= 64) {
        char* rec = ws + WS_KV + ((size_t)(gp - 64) * 64 + rt * 4 + kt4) * KVREC + sh * 336 + d * 16;
        typedef __attribute__((ext_vector_type(8))) short s16x8;
        *(s16x8*)rec = (s16x8){lo[0], lo[1], lo[2], lo[3], hi[0], hi[1], hi[2], hi[3]};
        if (d == 19) *(u32x4*)(rec + 16) = (u32x4){0x3f803f80u, 0x3f803f80u, 0x3f803f80u, 0x3f803f80u};
      }
    }
  }
}

constexpr int ATT_TPS = 8;
constexpr int ATT_STG = ATT_TPS * KVREC;
constexpr int ATT_NSTG = 64 / ATT_TPS;

__device__ __forceinline__ void att_issue(const char* kv, int stage, unsigned dst, int wave, unsigned voff) {
  const char* src = kv + (size_t)stage * ATT_STG;
#pragma unroll
  for (int i = 0; i < 5; ++i) glds16(src + (wave + 4 * i) * 1024, voff, dst + (wave + 4 * i) * 1024);
  if (wave == 0) glds16(src + 20 * 1024, voff, dst + 20 * 1024);
}

__device__ __forceinline__ void attention_role(int ab, char* __restrict__ ws, ldsp smem, int wave, int lane) {
  const int xg = ab & 7, idx = ab >> 3, head = 4 * xg + (idx & 3), qt = idx >> 2;
  const int r = lane & 31, hh = lane >> 5;
  const char* kv = ws + WS_KV + (size_t)head * 64 * KVREC;
  const unsigned lds0 = (unsigned)(uintptr_t)smem;
  const unsigned voff = lane * 16;
  att_issue(kv, 0, lds0, wave, voff);
  const char* qrec = ws + WS_Q + ((size_t)head * 64 + qt * 4 + wave) * QREC;
  const bf16x8 qm = *(const bf16x8*)(qrec + lane * 16);
  s16x4 qtl = *(const s16x4*)(qrec + 1024 + r * 8);
  if (hh) qtl = (s16x4){0, 0, 0, 0};
  const float* n2 = (const float*)(ws + WS_N2);
  float kmax = n2[(32 + head) * 32 + r];
#pragma unroll
  for (int o = 16; o >= 1; o >>= 1) kmax = fmaxf(kmax, __shfl_xor(kmax, o));
  const float qmax = fmaxf(n2[head * 32 + qt * 2], n2[head * 32 + qt * 2 + 1]);
  const bool safe = __builtin_amdgcn_readfirstlane((int)(qmax * kmax <= SAFE_BOUND * SAFE_BOUND)) != 0;

  const unsigned ka = KOFF + lane * 16, kta = KOFF + 1024 + r * 8, va = hh * 336 + r * 16;
  f32x16 negm;
#pragma unroll
  for (int j = 0; j < 16; ++j) negm[j] = 0.f;
  if (!safe) {
    float m = -INFINITY;
    for (int s = 0; s < ATT_NSTG; ++s) {
      wait_vm_bar<0>();
      if (s + 1 < ATT_NSTG) att_issue(kv, s + 1, lds0 + ((s + 1) & 1) * ATT_STG, wave, voff);
      ldsp sb = smem + (s & 1) * ATT_STG;
#pragma unroll 2
      for (int t = 0; t < ATT_TPS; ++t) {
        f32x16 sc = MFMA32(ldsA(sb + t * KVREC + ka), qm, (f32x16){});
        sc = MFMA8(*(const LAS s16x4*)(sb + t * KVREC + kta), qtl, sc);
#pragma unroll
        for (int j = 0; j < 16; ++j) m = fmaxf(m, sc[j]);
      }
    }
    m = fmaxf(m, __shfl_xor(m, 32));
#pragma unroll
    for (int j = 0; j < 16; ++j) negm[j] = -m;
    bar_lds();
    att_issue(kv, 0, lds0, wave, voff);
  }
  f32x16 o;
#pragma unroll
  for (int j = 0; j < 16; ++j) o[j] = 0.f;
  for (int s = 0; s < ATT_NSTG; ++s) {
    wait_vm_bar<0>();
    if (s + 1 < ATT_NSTG) att_issue(kv, s + 1, lds0 + ((s + 1) & 1) * ATT_STG, wave, voff);
    ldsp sb = smem + (s & 1) * ATT_STG;
    f32x16 sn = MFMA32(ldsA(sb + ka), qm, negm);
    sn = MFMA8(*(const LAS s16x4*)(sb + kta), qtl, sn);
#pragma unroll
    for (int t = 0; t < ATT_TPS; ++t) {
      f32x16 sc = sn;
      if (t + 1 < ATT_TPS) {
        sn = MFMA32(ldsA(sb + (t + 1) * KVREC + ka), qm, negm);
        sn = MFMA8(*(const LAS s16x4*)(sb + (t + 1) * KVREC + kta), qtl, sn);
      }
      const bf16x8 v0 = ldsA(sb + t * KVREC + va), v1 = ldsA(sb + t * KVREC + va + 672);
      bf16x8 p0, p1;
#pragma unroll
      for (int j = 0; j < 8; ++j) p0[j] = (__bf16)__builtin_amdgcn_exp2f(sc[j]);
#pragma unroll
      for (int j = 0; j < 8; ++j) p1[j] = (__bf16)__builtin_amdgcn_exp2f(sc[8 + j]);
      o = MFMA32(v0, p0, o);
      o = MFMA32(v1, p1, o);
    }
  }
  const float l = __shfl(o[8], r + 32);
  const float rl = 1.0f / l;
  u16* op = (u16*)(ws + WS_O) + (size_t)(qt * 128 + wave * 32 + r) * DM + head * DH + 4 * hh;
  *(u32x2*)(op) = (u32x2){pack2(o[0] * rl, o[1] * rl), pack2(o[2] * rl, o[3] * rl)};
  *(u32x2*)(op + 8) = (u32x2){pack2(o[4] * rl, o[5] * rl), pack2(o[6] * rl, o[7] * rl)};
  if (hh == 0) *(u32x2*)(op + 16) = (u32x2){pack2(o[8] * rl, o[9] * rl), pack2(o[10] * rl, o[11] * rl)};
}

struct cf { float re, im; };
__device__ __forceinline__ cf cmul(cf a, cf b) { return {a.re * b.re - a.im * b.im, a.re * b.im + a.im * b.re}; }
__device__ __forceinline__ cf cfma(cf a, cf b, cf c) {
  return {fmaf(a.re, b.re, fmaf(-a.im, b.im, c.re)), fmaf(a.re, b.im, fmaf(a.im, b.re, c.im))};
}

struct SsmIn { const u16 *x, *lre, *lim, *bre, *bim, *logdt; };

__device__ __forceinline__ void ssm_role(const SsmIn& in, char* __restrict__ ws, ldsp smem, int tid, int wave, int lane) {
  LAS int* sh = (LAS int*)smem;
  if (tid == 0) sh[0] = (int)__hip_atomic_fetch_add((gu32*)(ws + WS_CTR), 1u, __ATOMIC_RELAXED, __HIP_MEMORY_SCOPE_AGENT);
  __syncthreads();
  const int ticket = __builtin_amdgcn_readfirstlane(sh[0]);
  __syncthreads();
  const int c = ticket / 10, pt = ticket - c * 10;
  const int wm = wave >> 1, wn = wave & 1, r = lane & 31, hh = lane >> 5;

  unsigned aoff[2], boff[2];
  nk_offsets<32, 2>(aoff, wave, lane, [&](int row) {
    const int i = row & 31, t = (row & 64) + 32 * ((i >> 2) & 1) + 16 * ((row >> 5) & 1) + (i & 3) + 4 * (i >> 3);
    return (unsigned)(t * (DM * 2));
  });
  nk_offsets<32, 2>(boff, wave, lane, [&](int row) { return (unsigned)((((row >> 6) << 5) + (row & 31)) * (DM * 2)); });
  const char* abase = (const char*)in.x + (size_t)c * 128 * (DM * 2);
  const char* bb0 = (const char*)((wave & 1) ? in.bim : in.bre) + (size_t)pt * 64 * (DM * 2);
  f32x16 acc[2][2];
#pragma unroll
  for (int i = 0; i < 2; ++i)
#pragma unroll
    for (int j = 0; j < 2; ++j) acc[i][j] = (f32x16){};
  gemm_core<32, 3, false, 2, false, 20>(acc, smem, wave, lane, abase, aoff, [&](int s) { return bb0 + (size_t)s * 64; }, boff);

  const int p = pt * 64 + wn * 32 + r;
  const float lr = bf2f(in.lre[p]), li = bf2f(in.lim[p]);
  const float dt = rbf(expf(bf2f(in.logdt[p])));
  cf lam;
  {
    const float ea = expf(dt * lr);
    float sn, cs;
    sincosf(dt * li, &sn, &cs);
    lam = {ea * cs, ea * sn};
  }
  cf coef;
  {
    const float nr = lam.re - 1.0f, ni = lam.im, den = lr * lr + li * li;
    coef = {(nr * lr + ni * li) / den, (ni * lr - nr * li) / den};
  }
  cf e = {0.f, 0.f};
#pragma unroll
  for (int mt = 0; mt < 2; ++mt)
#pragma unroll
    for (int j = 0; j < 16; ++j) e = cfma(lam, e, (cf){acc[mt][0][j], acc[mt][1][j]});
  cf l32 = lam;
#pragma unroll
  for (int i = 0; i < 5; ++i) l32 = cmul(l32, l32);
  const cf l64 = cmul(l32, l32), l128 = cmul(l64, l64);
  bar_lds();
  LAS f32x2* ex = (LAS f32x2*)smem;
  LAS f32x2* part = ex + 256;
  const int sub = 2 * wm + hh, pl = wn * 32 + r;
  ex[sub * 64 + pl] = (f32x2){e.re, e.im};
  bar_lds();
  cf es[4];
#pragma unroll
  for (int j = 0; j < 4; ++j) { const f32x2 t = ex[j * 64 + pl]; es[j] = {t[0], t[1]}; }
  gu64* gran = (gu64*)(ws + WS_GRAN);
  if (sub == 0) {
    cf E = es[0];
#pragma unroll
    for (int j = 1; j < 4; ++j) E = cfma(l32, E, es[j]);
    gu64* g = gran + ((size_t)c * 640 + p) * 2;
    __hip_atomic_store(g, (1ull << 32) | __float_as_uint(E.re), __ATOMIC_RELAXED, __HIP_MEMORY_SCOPE_AGENT);
    __hip_atomic_store(g + 1, (1ull << 32) | __float_as_uint(E.im), __ATOMIC_RELAXED, __HIP_MEMORY_SCOPE_AGENT);
  }
  {
    const int pp = pt * 64 + lane;
    LAS f32x2* lx = part + 256;
    if (sub == 0) lx[pl] = (f32x2){l128.re, l128.im};
    bar_lds();
    const f32x2 t = lx[lane];
    const cf L1 = {t[0], t[1]}, L2 = cmul(L1, L1), L4 = cmul(L2, L2);
    cf a = {0.f, 0.f};
    int last = -1;
    for (int cc = wave; cc < c; cc += 4) {
      gu64* g = gran + ((size_t)cc * 640 + pp) * 2;
      unsigned long long x0, x1;
      for (;;) {
        x0 = __hip_atomic_load(g, __ATOMIC_RELAXED, __HIP_MEMORY_SCOPE_AGENT);
        x1 = __hip_atomic_load(g + 1, __ATOMIC_RELAXED, __HIP_MEMORY_SCOPE_AGENT);
        if (__all((x0 >> 32) == 1ull && (x1 >> 32) == 1ull)) break;
        __builtin_amdgcn_s_sleep(2);
      }
      a = cfma(L4, a, (cf){__uint_as_float((unsigned)x0), __uint_as_float((unsigned)x1)});
      last = cc;
    }
    const int ex3 = c - 1 - last;
    if (last >= 0) {
      if (ex3 & 1) a = cmul(a, L1);
      if (ex3 & 2) a = cmul(a, L2);
    }
    part[wave * 64 + lane] = (f32x2){a.re, a.im};
  }
  bar_lds();
  cf h;
  {
    cf hin = {0.f, 0.f};
#pragma unroll
    for (int j = 0; j < 4; ++j) { const f32x2 t = part[j * 64 + pl]; hin.re += t[0]; hin.im += t[1]; }
    h = hin;
#pragma unroll
    for (int j = 0; j < 3; ++j)
      if (j < sub) h = cfma(l32, h, es[j]);
  }
  bar_lds();
  const cf c2 = {2.f * coef.re, 2.f * coef.im};
  LAS u16* img = (LAS u16*)smem;
  constexpr int IP = 136;
#pragma unroll
  for (int mt = 0; mt < 2; ++mt)
#pragma unroll
    for (int j = 0; j < 16; ++j) {
      h = cfma(lam, h, (cf){acc[mt][0][j], acc[mt][1][j]});
      const cf y = cmul(c2, h);
      const int t = wm * 64 + hh * 32 + mt * 16 + j;
      img[t * IP + pl] = __builtin_bit_cast(u16, (__bf16)y.re);
      img[t * IP + 64 + pl] = __builtin_bit_cast(u16, (__bf16)(-y.im));
    }
  bar_lds();
  u16* hb = (u16*)(ws + WS_H);
#pragma unroll
  for (int i = 0; i < 8; ++i) {
    const int ch = tid & 15, t = (tid >> 4) + 16 * i;
    const u32x4 v = *(const LAS u32x4*)(img + t * IP + ch * 8);
    *(u32x4*)(hb + (size_t)(c * 128 + t) * (2 * DM) + (ch >> 3) * DM + pt * 64 + (ch & 7) * 8) = v;
  }
}

constexpr int K2_LDS = 3 * 16384;
constexpr int N_ATT = 512, N_SSM = 160;
__global__ __launch_bounds__(256, 3) void k2_attn_ssm(SsmIn in, char* __restrict__ ws) {
  extern __shared__ __attribute__((aligned(16))) char smem_[];
  ldsp smem = (ldsp)smem_;
  const int tid = threadIdx.x, lane = tid & 63, wave = __builtin_amdgcn_readfirstlane(tid >> 6);
  if (blockIdx.x < N_ATT) attention_role(blockIdx.x, ws, smem, wave, lane);
  else ssm_role(in, ws, smem, tid, wave, lane);
}

constexpr int K3_LDS = 4 * 32768;
struct K3In { const u16 *x, *cre, *cim, *dskip, *wo; };
__global__ __launch_bounds__(256, 1) void k3_out(K3In in, const char* __restrict__ ws, float* __restrict__ out) {
  extern __shared__ __attribute__((aligned(16))) char smem_[];
  ldsp smem = (ldsp)smem_;
  const int tid = threadIdx.x, lane = tid & 63, wave = __builtin_amdgcn_readfirstlane(tid >> 6);
  const int wm = wave >> 1, wn = wave & 1, r = lane & 31, hh = lane >> 5;
  const int b = blockIdx.x;
  if (b < 160) {
    const int rt = b / 10, ct = b - rt * 10;
    unsigned aoff[4], boff[2];
    nk_offsets<64, 4>(aoff, wave, lane, [&](int row) { return (unsigned)(row * (2 * DM * 2)); });
    nk_offsets<64, 2>(boff, wave, lane, [&](int row) { return (unsigned)(row * (DM * 2)); });
    const char* abase = ws + WS_H + (size_t)rt * 128 * (2 * DM * 2);
    const char* bre = (const char*)in.cre + (size_t)ct * 64 * (DM * 2);
    const char* bim = (const char*)in.cim + (size_t)ct * 64 * (DM * 2);
    f32x16 acc[2][1];
    acc[0][0] = (f32x16){}; acc[1][0] = (f32x16){};
    gemm_core<64, 4, false, 1, true, 20>(acc, smem, wave, lane, abase, aoff,
                                         [&](int s) { return (s < 10 ? bre : bim - 10 * 128) + (size_t)s * 128; }, boff);
#pragma unroll
    for (int mt = 0; mt < 2; ++mt) {
      const int m = rt * 128 + wm * 64 + mt * 32 + r;
#pragma unroll
      for (int g = 0; g < 4; ++g) {
        const int n = ct * 64 + wn * 32 + 8 * g + 4 * hh;
        const u32x2 xv = *(const u32x2*)(in.x + (size_t)m * DM + n), dv = *(const u32x2*)(in.dskip + n);
        f32x4 y;
        y[0] = acc[mt][0][4 * g + 0] + rbf(bflo(xv[0]) * bflo(dv[0]));
        y[1] = acc[mt][0][4 * g + 1] + rbf(bfhi(xv[0]) * bfhi(dv[0]));
        y[2] = acc[mt][0][4 * g + 2] + rbf(bflo(xv[1]) * bflo(dv[1]));
        y[3] = acc[mt][0][4 * g + 3] + rbf(bfhi(xv[1]) * bfhi(dv[1]));
        *(f32x4*)(out + (size_t)m * (2 * DM) + n) = y;
      }
    }
  } else {
    const int bb = b - 160, rt = bb / 5, ct = bb - rt * 5;
    unsigned aoff[4], boff[4];
    nk_offsets<64, 4>(aoff, wave, lane, [&](int row) { return (unsigned)(row * (DM * 2)); });
#pragma unroll
    for (int i = 0; i < 4; ++i) {
      const int row = (wave * 4 + i) * 4 + (lane >> 4), ph = lane & 15;
      boff[i] = row * (DM * 2) + (ph ^ ((row & 3) << 2)) * 16;
    }
    const char* abase = ws + WS_O + (size_t)rt * 128 * (DM * 2);
    const char* bb0 = (const char*)in.wo + ct * 256;
    f32x16 acc[2][2];
#pragma unroll
    for (int i = 0; i < 2; ++i)
#pragma unroll
      for (int j = 0; j < 2; ++j) acc[i][j] = (f32x16){};
    gemm_core<64, 4, true, 2, true, 10>(acc, smem, wave, lane, abase, aoff,
                                        [&](int s) { return bb0 + (size_t)s * (64 * DM * 2); }, boff);
#pragma unroll
    for (int mt = 0; mt < 2; ++mt) {
      const int m = rt * 128 + wm * 64 + mt * 32 + r;
#pragma unroll
      for (int nt = 0; nt < 2; ++nt)
#pragma unroll
        for (int g = 0; g < 4; ++g) {
          const int n = ct * 128 + wn * 64 + nt * 32 + 8 * g + 4 * hh;
          f32x4 y;
#pragma unroll
          for (int j = 0; j < 4; ++j) y[j] = rbf(acc[mt][nt][4 * g + j]);
          *(f32x4*)(out + (size_t)m * (2 * DM) + DM + n) = y;
        }
    }
  }
}

extern "C" void kernel_launch(void* const* d_in, const int* in_sizes, int n_in, void* d_out, int out_size, void* d_ws,
                              size_t ws_size, hipStream_t stream) {
  const u16* A_x = (const u16*)d_in[0];
  const u16* lre = (const u16*)d_in[1];
  const u16* lim = (const u16*)d_in[2];
  const u16* bre = (const u16*)d_in[3];
  const u16* bim = (const u16*)d_in[4];
  const u16* cre = (const u16*)d_in[5];
  const u16* cim = (const u16*)d_in[6];
  const u16* logdt = (const u16*)d_in[7];
  const u16* dskip = (const u16*)d_in[8];
  const u16* B_x = (const u16*)d_in[9];
  const u16* wqkv = (const u16*)d_in[10];
  const u16* wo = (const u16*)d_in[11];
  char* ws = (char*)d_ws;
  (void)hipFuncSetAttribute((const void*)k1_qkv, hipFuncAttributeMaxDynamicSharedMemorySize, K1_LDS);
  (void)hipFuncSetAttribute((const void*)k3_out, hipFuncAttributeMaxDynamicSharedMemorySize, K3_LDS);
  k1_qkv<<<256, 256, K1_LDS, stream>>>(B_x, wqkv, ws);
  SsmIn sin = {A_x, lre, lim, bre, bim, logdt};
  k2_attn_ssm<<<N_ATT + N_SSM, 256, K2_LDS, stream>>>(sin, ws);
  K3In kin = {A_x, cre, cim, dskip, wo};
  k3_out<<<240, 256, K3_LDS, stream>>>(kin, ws, (float*)d_out);
}
```

```cpp
#include <hip/hip_runtime.h>
#include <hip/hip_bf16.h>
#include <stdint.h>

typedef __attribute__((ext_vector_type(8))) __bf16 bf16x8;
typedef __attribute__((ext_vector_type(4))) short s16x4;
typedef __attribute__((ext_vector_type(8))) short s16x8;
typedef __attribute__((ext_vector_type(16))) float f32x16;
typedef __attribute__((ext_vector_type(4))) float f32x4;
typedef __attribute__((ext_vector_type(2))) float f32x2;
typedef __attribute__((ext_vector_type(4))) unsigned u32x4;
typedef __attribute__((ext_vector_type(2))) unsigned u32x2;
typedef unsigned short u16;
#define LAS __attribute__((address_space(3)))
#define GAS __attribute__((address_space(1)))
typedef LAS char* ldsp;
typedef GAS unsigned long long gu64;
typedef GAS unsigned gu32;

constexpr int SEQ = 2048, DM = 640, NH = 32, DH = 20;
constexpr int VSEG = 352, KOFF = 4 * VSEG, KVREC = KOFF + 1280;
constexpr int QREC = 1280;

constexpr size_t WS_CNT = 0;
constexpr size_t WS_GRAN = 4096;
constexpr size_t WS_ZERO_BYTES = WS_GRAN + 16 * 640 * 2 * 8;
constexpr size_t WS_N2 = 262144;
constexpr size_t WS_TAB = WS_N2 + 16384;
constexpr size_t WS_KV = WS_TAB + 32768;
constexpr size_t WS_Q = WS_KV + (size_t)NH * 64 * KVREC;
constexpr size_t WS_O = WS_Q + (size_t)NH * 64 * QREC;
constexpr size_t WS_H = WS_O + (size_t)SEQ * DM * 2;
constexpr size_t WS_END = WS_H + (size_t)SEQ * 2 * DM * 2;

constexpr float QSCALE = 0.22360679774997896f * 1.4426950408889634f;
constexpr float SAFE_BOUND = 40.0f;

#define STAMP_DECL
#define STAMP(i) do {} while (0)
#define STAMP_END(ws, kid) do {} while (0)
#define STAMP_ARG
#define STAMP_PASS

#define SBAR() __builtin_amdgcn_sched_barrier(0)
#define PIN(x) asm volatile("" : "+v"(x))
#define MFMA32(a, b, c) __builtin_amdgcn_mfma_f32_32x32x16_bf16(a, b, c, 0, 0, 0)
#define MFMA8(a, b, c) __builtin_amdgcn_mfma_f32_32x32x8bf16_1k(a, b, c, 0, 0, 0)
#define RLX_AGENT __ATOMIC_RELAXED, __HIP_MEMORY_SCOPE_AGENT

__device__ __forceinline__ void glds16(const void* sbase, unsigned voff, unsigned lds_addr) {
  unsigned keep;
  asm volatile("s_mov_b32 %0, m0\n\ts_mov_b32 m0, %3\n\ts_nop 0\n\tglobal_load_lds_dwordx4 %1, %2\n\ts_mov_b32 m0, %0"
               : "=&s"(keep) : "v"(voff), "s"(sbase), "s"(lds_addr) : "memory");
}
template <int N> __device__ __forceinline__ void wait_vm_bar() {
  asm volatile("s_waitcnt vmcnt(%0) lgkmcnt(0)\n\ts_barrier" ::"i"(N) : "memory");
}
__device__ __forceinline__ void bar_lds() { asm volatile("s_waitcnt lgkmcnt(0)\n\ts_barrier" ::: "memory"); }
__device__ __forceinline__ float bf2f(u16 v) { return __uint_as_float((unsigned)v << 16); }
__device__ __forceinline__ float bflo(unsigned v) { return __uint_as_float(v << 16); }
__device__ __forceinline__ float bfhi(unsigned v) { return __uint_as_float(v & 0xffff0000u); }
__device__ __forceinline__ unsigned pack2(float lo, float hi) {
  typedef __attribute__((ext_vector_type(2))) __bf16 bf16x2;
  bf16x2 r; r[0] = (__bf16)lo; r[1] = (__bf16)hi;
  return __builtin_bit_cast(unsigned, r);
}
__device__ __forceinline__ float wave_max_nonneg(float v) {
  int x = __float_as_int(v);
  x = max(x, __builtin_amdgcn_update_dpp(0, x, 0x111, 0xf, 0xf, true));
  x = max(x, __builtin_amdgcn_update_dpp(0, x, 0x112, 0xf, 0xf, true));
  x = max(x, __builtin_amdgcn_update_dpp(0, x, 0x114, 0xf, 0xf, true));
  x = max(x, __builtin_amdgcn_update_dpp(0, x, 0x118, 0xf, 0xf, true));
  x = max(x, __builtin_amdgcn_update_dpp(0, x, 0x142, 0xa, 0xf, true));
  x = max(x, __builtin_amdgcn_update_dpp(0, x, 0x143, 0xc, 0xf, true));
  return __int_as_float(x);
}
__device__ __forceinline__ float rbf(float v) { return (float)(__bf16)v; }
__device__ __forceinline__ bf16x8 ldsA(ldsp p) { return *(const LAS bf16x8*)p; }
__device__ __forceinline__ s16x4 ldstr(ldsp p) {
  typedef short v4i16_t __attribute__((ext_vector_type(4)));
  return __builtin_bit_cast(s16x4, __builtin_amdgcn_ds_read_tr16_b64_v4i16((LAS v4i16_t*)p));
}

__device__ __forceinline__ int swz64(int row) { return (row >> 1) & 7; }

template <int MT, int NT, int NSA, int NSB> struct GemmGeo {
  static constexpr int ASZ = 64 * MT * 128, BSZ = 64 * NT * 128, BOFF = NSA * ASZ, BYTES = BOFF + NSB * BSZ;
  static constexpr int NA = 2 * MT, NB = 2 * NT;
};

template <int NSA, int NSB, int MT, int NT, int NK, class BBase>
__device__ __forceinline__ void gemm_loader(ldsp smem, int lw, const char* abase, const unsigned (&aoff)[2 * MT], BBase bbase,
                                            const unsigned (&boff)[2 * NT]) {
  typedef GemmGeo<MT, NT, NSA, NSB> G;
  static_assert(NSB == NSA || NSB == NSA - 1, "ring shapes");
  constexpr int DA = NSA - 1, DB = NSB - 1, NA = G::NA, NB = G::NB;
  const unsigned lds0 = (unsigned)(uintptr_t)smem;
  auto issueA = [&](int stage, int slot) {
    const char* ab = abase + (size_t)stage * 128;
#pragma unroll
    for (int i = 0; i < NA; ++i) glds16(ab, aoff[i], lds0 + slot * G::ASZ + (lw * NA + i) * 1024);
  };
  auto issueB = [&](int stage, int slot) {
    const char* bb = bbase(stage);
#pragma unroll
    for (int i = 0; i < NB; ++i) glds16(bb, boff[i], lds0 + G::BOFF + slot * G::BSZ + (lw * NB + i) * 1024);
  };
#pragma unroll
  for (int s = 0; s < DA; ++s) {
    issueA(s, s);
    if (s < DB) issueB(s, s);
  }
  int sa = NSA - 1, sb = NSB - 1;
  for (int kt = 0; kt < NK; ++kt) {
    const int rem = NK - 1 - kt;
    if constexpr (NSB == NSA) {
      if (rem >= DA - 1) wait_vm_bar<(DA - 1) * (NA + NB)>();
      else if (DA > 2 && rem == 1) wait_vm_bar<NA + NB>();
      else wait_vm_bar<0>();
    } else {
      static_assert(NSB == NSA || NSA == 3, "asymmetric ring: 3 + 2 slots");
      if (rem >= 1) wait_vm_bar<NA>(); else wait_vm_bar<0>();
    }
    if (kt + DB < NK) issueB(kt + DB, sb);
    if (kt + DA < NK) issueA(kt + DA, sa);
    sa = (sa + 1 == NSA) ? 0 : sa + 1;
    sb = (sb + 1 == NSB) ? 0 : sb + 1;
  }
}

template <int NSA, int NSB, bool BKN, int MT, int NT, bool SWAP, int NK>
__device__ __forceinline__ void gemm_consumer(f32x16 (&acc)[MT][NT], ldsp smem, int wave, int lane STAMP_ARG) {
  typedef GemmGeo<MT, NT, NSA, NSB> G;
  static_assert(!BKN || NT == 2, "transposed B image is 128 columns wide");
  const int wm = wave >> 1, wn = wave & 1, r = lane & 31, hh = lane >> 5;
  unsigned fa[4], fb[BKN ? NT : 1];
#pragma unroll
  for (int ks = 0; ks < 4; ++ks) fa[ks] = r * 128 + (((2 * ks + hh) ^ swz64(r)) << 4);
  if constexpr (BKN) {
    const int nh = (lane >> 4) & 1, q = (lane & 15) >> 2, p = lane & 3;
#pragma unroll
    for (int nt = 0; nt < NT; ++nt)
      fb[nt] = (8 * hh + q) * 256 + (((((2 * wn + nt) ^ q) << 2) + 2 * nh + (p >> 1)) << 4) + (p & 1) * 8;
  } else fb[0] = 0;
  bf16x8 fA[2][MT], fB[2][NT];
  auto loadf = [&](int set, ldsp pa, ldsp pb, int ks) {
#pragma unroll
    for (int mt = 0; mt < MT; ++mt) fA[set][mt] = ldsA(pa + fa[ks] + (wm * 32 * MT + mt * 32) * 128);
#pragma unroll
    for (int nt = 0; nt < NT; ++nt) {
      if constexpr (BKN) {
        const s16x4 lo = ldstr(pb + fb[nt] + ks * 16 * 256), hi = ldstr(pb + fb[nt] + (ks * 16 + 4) * 256);
        const s16x8 v = {lo[0], lo[1], lo[2], lo[3], hi[0], hi[1], hi[2], hi[3]};
        fB[set][nt] = __builtin_bit_cast(bf16x8, v);
      } else {
        fB[set][nt] = ldsA(pb + fa[ks] + (wn * 32 * NT + nt * 32) * 128);
      }
    }
  };
  STAMP(1);
  bar_lds();
  STAMP(2);
  loadf(0, smem, smem + G::BOFF, 0);
  int sa = 0, sb = 0;
  for (int kt = 0; kt < NK; ++kt) {
    ldsp pa = smem + sa * G::ASZ, pb = smem + G::BOFF + sb * G::BSZ;
    sa = (sa + 1 == NSA) ? 0 : sa + 1;
    sb = (sb + 1 == NSB) ? 0 : sb + 1;
#pragma unroll
    for (int ks = 0; ks < 4; ++ks) {
      if (ks < 3) {
        loadf((ks + 1) & 1, pa, pb, ks + 1);
      } else if (kt + 1 < NK) {
        bar_lds();
        loadf(0, smem + sa * G::ASZ, smem + G::BOFF + sb * G::BSZ, 0);
      }
      SBAR();
#pragma unroll
      for (int mt = 0; mt < MT; ++mt)
#pragma unroll
        for (int nt = 0; nt < NT; ++nt)
          acc[mt][nt] = SWAP ? MFMA32(fB[ks & 1][nt], fA[ks & 1][mt], acc[mt][nt]) : MFMA32(fA[ks & 1][mt], fB[ks & 1][nt], acc[mt][nt]);
      SBAR();
    }
  }
  STAMP(3);
}

template <int N, class RowFn>
__device__ __forceinline__ void nk_offsets(unsigned (&off)[N], int lw, int lane, RowFn rowfn) {
#pragma unroll
  for (int i = 0; i < N; ++i) {
    const int row = (lw * N + i) * 8 + (lane >> 3);
    off[i] = rowfn(row) + (((lane & 7) ^ swz64(row)) << 4);
  }
}
__device__ __forceinline__ void kn_offsets(unsigned (&off)[4], int lw, int lane, unsigned pitch, int cmax) {
#pragma unroll
  for (int i = 0; i < 4; ++i) {
    const int row = (lw * 4 + i) * 4 + (lane >> 4);
    const int c = min((lane & 15) ^ ((row & 3) << 2), cmax);
    off[i] = row * pitch + c * 16;
  }
}
template <int MT, int NT> __device__ __forceinline__ void zero_acc(f32x16 (&acc)[MT][NT]) {
#pragma unroll
  for (int i = 0; i < MT; ++i)
#pragma unroll
    for (int j = 0; j < NT; ++j) acc[i][j] = (f32x16){};
}

constexpr int K1_LDS = 4 * 32768;
struct cf { float re, im; };
__device__ __forceinline__ cf cmul(cf a, cf b) { return {a.re * b.re - a.im * b.im, a.re * b.im + a.im * b.re}; }
__device__ __forceinline__ cf cfma(cf a, cf b, cf c) {
  return {fmaf(a.re, b.re, fmaf(-a.im, b.im, c.re)), fmaf(a.re, b.im, fmaf(a.im, b.re, c.im))};
}
struct K1In { const u16 *x, *w, *lre, *lim, *logdt; };
__global__ __launch_bounds__(512, 2) void k1_qkv(K1In in, char* __restrict__ ws) {
  const u16* __restrict__ x = in.x;
  const u16* __restrict__ w = in.w;
  extern __shared__ __attribute__((aligned(16))) char smem_[];
  ldsp smem = (ldsp)smem_;
  const int tid = threadIdx.x, lane = tid & 63, wave = __builtin_amdgcn_readfirstlane(tid >> 6);
  STAMP_DECL STAMP(0);
  const int b = blockIdx.x, xcd = b & 7, idx = b >> 3;
  const int rt = 4 * (xcd & 3) + (idx & 3), ct = 8 * (xcd >> 2) + (idx >> 2);
  constexpr int CP = 264;
  if (wave >= 4) {
    unsigned aoff[4], boff[4];
    nk_offsets<4>(aoff, wave - 4, lane, [&](int row) { return (unsigned)(row * (DM * 2)); });
    kn_offsets(boff, wave - 4, lane, 3 * DM * 2, 14);
    const char* bb0 = (const char*)w + ct * 240;
    gemm_loader<4, 4, 2, 2, 10>(smem, wave - 4, (const char*)x + (size_t)rt * 128 * (DM * 2), aoff,
                             [&](int s) { return bb0 + (size_t)s * (64 * 3 * DM * 2); }, boff);
    bar_lds();
  } else {
    if (tid < 41) *(u32x4*)(ws + (size_t)b * 656 + tid * 16) = (u32x4){0u, 0u, 0u, 0u};
    if (b < 10 && wave == 0) {
      const int p = b * 64 + lane;
      const float lr = bf2f(in.lre[p]), li = bf2f(in.lim[p]);
      const float dt = rbf(expf(bf2f(in.logdt[p])));
      const float ea = expf(dt * lr);
      float sn, cs;
      sincosf(dt * li, &sn, &cs);
      const cf lam = {ea * cs, ea * sn};
      const float nr = lam.re - 1.0f, ni = lam.im, den = lr * lr + li * li;
      cf l32 = lam;
#pragma unroll
      for (int i = 0; i < 5; ++i) l32 = cmul(l32, l32);
      const cf l64 = cmul(l32, l32), l128 = cmul(l64, l64);
      f32x4* t = (f32x4*)(ws + WS_TAB) + p * 2;
      t[0] = (f32x4){lam.re, lam.im, 2.f * (nr * lr + ni * li) / den, 2.f * (ni * lr - nr * li) / den};
      t[1] = (f32x4){l32.re, l32.im, l128.re, l128.im};
    }
    f32x16 acc[2][2];
    zero_acc(acc);
    gemm_consumer<4, 4, true, 2, 2, true, 10>(acc, smem, wave, lane STAMP_PASS);
    bar_lds();
    const int wm = wave >> 1, wn = wave & 1, r = lane & 31, hh = lane >> 5;
#pragma unroll
    for (int mt = 0; mt < 2; ++mt)
#pragma unroll
      for (int nt = 0; nt < 2; ++nt)
#pragma unroll
        for (int g = 0; g < 4; ++g) {
          const u32x2 v = {pack2(acc[mt][nt][4 * g], acc[mt][nt][4 * g + 1]), pack2(acc[mt][nt][4 * g + 2], acc[mt][nt][4 * g + 3])};
          *(LAS u32x2*)(smem + (wm * 64 + mt * 32 + r) * CP + (wn * 64 + nt * 32 + 8 * g + 4 * hh) * 2) = v;
        }
  }
  bar_lds();
  STAMP(4);
  float* n2 = (float*)(ws + WS_N2);
  {
    const int row = tid & 127, grow = rt * 128 + row, t32 = grow >> 5, rr = grow & 31;
    unsigned v[2][10];
#pragma unroll
    for (int i = 0; i < 2; ++i) {
      const int part = min((wave >> 1) + 4 * i, 5);
#pragma unroll
      for (int j = 0; j < 5; ++j) {
        const u32x2 t = *(const LAS u32x2*)(smem + row * CP + part * 40 + j * 8);
        v[i][2 * j] = t[0]; v[i][2 * j + 1] = t[1];
      }
    }
#pragma unroll
    for (int i = 0; i < 2; ++i) {
      const int part = (wave >> 1) + 4 * i, gp = 6 * ct + part, type = gp >> 5, head = gp & 31;
      if (part < 6 && type < 2) {
        if (type == 0) {
#pragma unroll
          for (int j = 0; j < 10; ++j) v[i][j] = pack2(bflo(v[i][j]) * QSCALE, bfhi(v[i][j]) * QSCALE);
        }
        char* rec = type == 0 ? ws + WS_Q + ((size_t)head * 64 + t32) * QREC : ws + WS_KV + ((size_t)head * 64 + t32) * KVREC + KOFF;
        *(u32x4*)(rec + rr * 16) = (u32x4){v[i][0], v[i][1], v[i][2], v[i][3]};
        *(u32x4*)(rec + 512 + rr * 16) = (u32x4){v[i][4], v[i][5], v[i][6], v[i][7]};
        *(u32x2*)(rec + 1024 + rr * 8) = (u32x2){v[i][8], v[i][9]};
        float ss = 0.f, s2 = 0.f;
#pragma unroll
        for (int j = 0; j < 10; ++j) { const float a = bflo(v[i][j]), c = bfhi(v[i][j]); ss = fmaf(a, a, ss); s2 = fmaf(c, c, s2); }
        ss = wave_max_nonneg(ss + s2);
        if (lane == 63) n2[(type * 32 + head) * 32 + (grow >> 6)] = ss;
      }
    }
  }
  STAMP(5);
  if (6 * ct + 5 >= 64) {
    const int q = (lane & 15) >> 2, p = lane & 3, i16 = lane & 15, grp = tid >> 4;
#pragma unroll
    for (int it = 0; it < 4; ++it) {
      const int item = it * 32 + grp;
      const int cg = item & 7, kt4 = (item >> 3) & 3, sh = item >> 5, s = sh >> 1, h2 = sh & 1;
      const int row0 = 32 * kt4 + 16 * s + 4 * h2 + q;
      ldsp a = smem + row0 * CP + (16 * cg + 4 * p) * 2;
      const s16x4 lo = ldstr(a), hi = ldstr(a + 8 * CP);
      const int c = 16 * cg + i16, part = c / 20, d = c - part * 20, gp = 6 * ct + part;
      if (c < 120 && gp >= 64) {
        char* rec = ws + WS_KV + ((size_t)(gp - 64) * 64 + rt * 4 + kt4) * KVREC + sh * VSEG + d * 16;
        *(s16x8*)rec = (s16x8){lo[0], lo[1], lo[2], lo[3], hi[0], hi[1], hi[2], hi[3]};
        if (d == 19) *(u32x4*)(rec + 16) = (u32x4){0x3f803f80u, 0x3f803f80u, 0x3f803f80u, 0x3f803f80u};
        if (d == 18) *(u32x4*)(rec + 48) = (u32x4){0u, 0u, 0u, 0u};
      }
    }
  }
  STAMP_END(ws, 0);
}

#define ATT_PRIO 3
#define EXP_W 0
#define ATT_FLIP 6
#define ATT_DMA_T 0
constexpr int ATT_HALF = 4 * KVREC;
constexpr int ATT_STG = 2 * ATT_HALF;
constexpr int ATT_NSTG = 8;

struct AttDma {
  const char* kv; unsigned lds0, vlin, vmid;
  static constexpr int DELTA = 32 * KVREC - ATT_HALF;
  __device__ __forceinline__ void piece(int stage, int buf, int pc) const {
    glds16(kv + (size_t)stage * ATT_HALF + pc * 1024 + (pc > 10 ? DELTA : 0), pc == 10 ? vmid : vlin,
           __builtin_amdgcn_readfirstlane(lds0 + buf * ATT_STG + pc * 1024));
  }
  __device__ __forceinline__ void warm(int stage, int wave) const {
#pragma unroll
    for (int i = 0; i < 3; ++i) {
      const int pc = wave + 8 * i;
      if (pc < 21) glds16(kv + (size_t)stage * ATT_HALF + pc * 1024 + (pc > 10 ? DELTA : 0), pc == 10 ? vmid : vlin,
                          __builtin_amdgcn_readfirstlane(lds0 + 3 * ATT_STG + 3072));
    }
  }
  unsigned v3[3], pc3;
  __device__ __forceinline__ void setup3(int wave) {
    const int p0 = 3 * min(wave, 6);
    pc3 = (p0 + 1) * 1024;
#pragma unroll
    for (int i = 0; i < 3; ++i) v3[i] = p0 + i == 10 ? vmid : (p0 + i > 10 ? vlin + DELTA : vlin);
  }
  __device__ __forceinline__ void three(int stage, int buf) const {
    const char* base = kv + (size_t)stage * ATT_HALF + pc3;
    const unsigned dst = __builtin_amdgcn_readfirstlane(lds0 + buf * ATT_STG + pc3);
    unsigned keep;
    asm volatile("s_mov_b32 %0, m0\n\ts_mov_b32 m0, %5\n\ts_nop 0\n\t"
                 "global_load_lds_dwordx4 %1, %4 offset:-1024\n\tglobal_load_lds_dwordx4 %2, %4\n\t"
                 "global_load_lds_dwordx4 %3, %4 offset:1024\n\ts_mov_b32 m0, %0"
                 : "=&s"(keep) : "v"(v3[0]), "v"(v3[1]), "v"(v3[2]), "s"(base), "s"(dst) : "memory");
  }
};

__device__ __forceinline__ void attention_role(int ab, char* __restrict__ ws, ldsp smem, int wave, int lane STAMP_ARG) {
  const int xg = ab & 7, idx = ab >> 3, head = 4 * xg + (idx & 3), qt = idx >> 2;
  const int r = lane & 31, hh = lane >> 5, qg = wave & 3, kh = wave >> 2;
  AttDma dma;
  dma.kv = ws + WS_KV + (size_t)head * 64 * KVREC;
  dma.lds0 = (unsigned)(uintptr_t)smem;
  dma.vlin = lane * 16;
  dma.vmid = lane * 16 + (lane >= 32 ? AttDma::DELTA : 0);
  dma.setup3(wave);
  dma.three(0, 0);
  const char* qrec = ws + WS_Q + ((size_t)head * 64 + qt * 8 + qg * 2) * QREC;
  const bf16x8 qm0 = *(const bf16x8*)(qrec + lane * 16), qm1 = *(const bf16x8*)(qrec + QREC + lane * 16);
  s16x4 qt0 = *(const s16x4*)(qrec + 1024 + r * 8), qt1 = *(const s16x4*)(qrec + QREC + 1024 + r * 8);
  if (hh) { qt0 = (s16x4){0, 0, 0, 0}; qt1 = qt0; }
  const float* n2 = (const float*)(ws + WS_N2);
  float kmax = n2[(32 + head) * 32 + r];
#pragma unroll
  for (int o = 16; o >= 1; o >>= 1) kmax = fmaxf(kmax, __shfl_xor(kmax, o));
  const float qmax = n2[head * 32 + qt * 4 + qg];
  dma.three(1, 1);
  if (qt >= 2) dma.warm(qt, wave);
  const bool safe = __all(qmax * kmax <= SAFE_BOUND * SAFE_BOUND) != 0;

  const unsigned hb = kh * ATT_HALF;
  const unsigned ka = hb + KOFF + lane * 16, va = hb + hh * VSEG + min(r, 21) * 16;
  unsigned kta = hb + (hh ? 21 * 16 : KOFF + 1024 + r * 8);
  LAS int* flag = (LAS int*)(smem + 3 * ATT_STG);
  if (wave == 0 && lane == 0) flag[0] = 0;
  wait_vm_bar<0>();
  if (!safe && lane == 0) flag[0] = 1;
  bar_lds();
  const bool slow = __builtin_amdgcn_readfirstlane(flag[0]) != 0;
  if (slow) {
    float m0 = -INFINITY, m1 = -INFINITY;
    for (int s = 0; s < ATT_NSTG; ++s) {
      ldsp sb = smem + (s & 1) * ATT_STG;
#pragma unroll 1
      for (int t = 0; t < 4; ++t) {
        const bf16x8 k = ldsA(sb + t * KVREC + ka);
        const s16x4 kt = *(const LAS s16x4*)(sb + t * KVREC + kta);
        f32x16 sc = MFMA32(k, qm0, (f32x16){});
        sc = MFMA8(kt, qt0, sc);
#pragma unroll
        for (int j = 0; j < 16; ++j) m0 = fmaxf(m0, sc[j]);
        sc = MFMA32(k, qm1, (f32x16){});
        sc = MFMA8(kt, qt1, sc);
#pragma unroll
        for (int j = 0; j < 16; ++j) m1 = fmaxf(m1, sc[j]);
      }
      bar_lds();
      dma.three((s + 2) & (ATT_NSTG - 1), s & 1);
      if (s + 1 < ATT_NSTG) wait_vm_bar<3>(); else wait_vm_bar<0>();
    }
    m0 = fmaxf(m0, __shfl_xor(m0, 32));
    m1 = fmaxf(m1, __shfl_xor(m1, 32));
    LAS f32x2* mx = (LAS f32x2*)(smem + 3 * ATT_STG + 16);
    if (hh == 0) mx[wave * 32 + r] = (f32x2){m0, m1};
    bar_lds();
    const f32x2 om = mx[(wave ^ 4) * 32 + r];
    m0 = fmaxf(m0, om[0]);
    m1 = fmaxf(m1, om[1]);
    if (hh) {
      const float h0 = rbf(m0), h1 = rbf(m1);
      const unsigned w0 = pack2(-h0, h0 - m0), w1 = pack2(-h1, h1 - m1);
      qt0 = (s16x4){(short)(w0 & 0xffff), (short)(w0 >> 16), 0, 0};
      qt1 = (s16x4){(short)(w1 & 0xffff), (short)(w1 >> 16), 0, 0};
      kta = hb + 20 * 16;
    }
  }
  f32x16 o0, o1;
#pragma unroll
  for (int j = 0; j < 16; ++j) { o0[j] = 0.f; o1[j] = 0.f; }
  STAMP(2);
  f32x16 sA, sB;
  bf16x8 kf, v0, v1;
  s16x4 kt;
  u32x4 p0, p1 = {0u, 0u, 0u, 0u};
  const f32x16 zero = {};
  kf = ldsA(smem + ka); kt = *(const LAS s16x4*)(smem + kta);
  v0 = ldsA(smem + va);
  v1 = v0;
  sA = MFMA32(kf, qm0, zero);
  sA = MFMA8(kt, qt0, sA);
#define ATT_BAR() wait_vm_bar<0>()
#define ATT_DMA_ON 1
#define DSTAMP(i) do {} while (0)
#define EX(x) __builtin_amdgcn_exp2f(x)
#define LDK(base, J) do { kf = ldsA((base) + (J) * KVREC + ka); kt = *(const LAS s16x4*)((base) + (J) * KVREC + kta); } while (0)
#define ATT_STEP(SC, SN, QM, QT, OP, OC, T)                                                             \
  {                                                                                                     \
    float e0, e1, e2, e3, e4, e5, e6, e7;                                                               \
            \
    if (ATT_PRIO == 3) { if ((T) == 0) { if (kh) __builtin_amdgcn_s_setprio(1); }                       \
                         else if ((T) == ATT_FLIP) { if (kh) __builtin_amdgcn_s_setprio(0); } }         \
    e0 = EX(SC[0]); e1 = EX(SC[1]);                                                                     \
    PIN(e0); PIN(e1); SBAR();                                                                           \
    OP = MFMA32(v1, __builtin_bit_cast(bf16x8, p1), OP);                                                \
    if (((T) & 1) == 0) v1 = ldsA(sb + ((T) >> 1) * KVREC + va + 2 * VSEG);                             \
    e2 = EX(SC[2]); e3 = EX(SC[3]);                                                                     \
    PIN(e2); PIN(e3); SBAR();                                                                           \
                                      \
    if ((T) == ATT_DMA_T && ATT_DMA_ON) dma.three(min(s + 2, ATT_NSTG - 1), bf2);    \
    SN = MFMA32(kf, QM, zero);                                                                          \
    e4 = EX(SC[4]); e5 = EX(SC[5]); p0[0] = pack2(e0, e1); e6 = EX(SC[6]); e7 = EX(SC[7]);              \
    p0[1] = pack2(e2, e3);                                                                              \
    PIN(e4); PIN(e5); PIN(e6); PIN(e7); PIN(p0); SBAR();                                                \
    SN = MFMA8(kt, QT, SN);                                                                             \
    p0[2] = pack2(e4, e5); e0 = EX(SC[8]); p0[3] = pack2(e6, e7); e1 = EX(SC[9]);                       \
    e2 = EX(SC[10]); e3 = EX(SC[11]);                                                                   \
    PIN(e0); PIN(e1); PIN(e2); PIN(e3); PIN(p0); SBAR();                                                \
    if ((T) == 6) {                                                                                     \
      DSTAMP(3);                                                                                        \
                         \
      if (ATT_DMA_ON) wait_vm_bar<3>(); else ATT_BAR();                                                 \
      DSTAMP(5);                                                                                        \
      LDK(sn, 0);                                                                                       \
    } else if (((T) & 1) == 0) LDK(sb, ((T) >> 1) + 1);                                                 \
    SBAR();                                                                                             \
    OC = MFMA32(v0, __builtin_bit_cast(bf16x8, p0), OC);                                                \
    if ((T) == 7) v0 = ldsA(sn + va);                                                                   \
    else if ((T) & 1) v0 = ldsA(sb + (((T) >> 1) + 1) * KVREC + va);                                    \
    e4 = EX(SC[12]); e5 = EX(SC[13]); e6 = EX(SC[14]); e7 = EX(SC[15]);                                 \
    p1[0] = pack2(e0, e1); p1[1] = pack2(e2, e3); p1[2] = pack2(e4, e5); p1[3] = pack2(e6, e7);         \
    PIN(p1); SBAR();                                                                                    \
  }
  int bf0 = 0;
  for (int s = 0; s < ATT_NSTG; ++s) {
    const int bf1 = bf0 == 2 ? 0 : bf0 + 1, bf2 = bf1 == 2 ? 0 : bf1 + 1;
    ldsp sb = smem + bf0 * ATT_STG;
    ldsp sn = smem + bf1 * ATT_STG;
    bf0 = bf1;
    ATT_STEP(sA, sB, qm1, qt1, o1, o0, 0)
    ATT_STEP(sB, sA, qm0, qt0, o0, o1, 1)
    ATT_STEP(sA, sB, qm1, qt1, o1, o0, 2)
    ATT_STEP(sB, sA, qm0, qt0, o0, o1, 3)
    ATT_STEP(sA, sB, qm1, qt1, o1, o0, 4)
    ATT_STEP(sB, sA, qm0, qt0, o0, o1, 5)
    ATT_STEP(sA, sB, qm1, qt1, o1, o0, 6)
    ATT_STEP(sB, sA, qm0, qt0, o0, o1, 7)
  }
  o1 = MFMA32(v1, __builtin_bit_cast(bf16x8, p1), o1);
#undef ATT_STEP
#undef LDK
#undef EX
  STAMP(3);
  wait_vm_bar<0>();
  LAS float* xch = (LAS float*)smem;
#pragma unroll
  for (int j = 0; j < 12; ++j) xch[(wave * 12 + j) * 64 + lane] = kh ? o0[j] : o1[j];
  bar_lds();
  float of[12];
#pragma unroll
  for (int j = 0; j < 12; ++j) of[j] = (kh ? o1[j] : o0[j]) + xch[((wave ^ 4) * 12 + j) * 64 + lane];
  const float rl = 1.0f / __shfl(of[8], r + 32);
  u16* op = (u16*)(ws + WS_O) + (size_t)(qt * 256 + qg * 64 + kh * 32 + r) * DM + head * DH + 4 * hh;
  *(u32x2*)(op) = (u32x2){pack2(of[0] * rl, of[1] * rl), pack2(of[2] * rl, of[3] * rl)};
  *(u32x2*)(op + 8) = (u32x2){pack2(of[4] * rl, of[5] * rl), pack2(of[6] * rl, of[7] * rl)};
  if (hh == 0) *(u32x2*)(op + 16) = (u32x2){pack2(of[8] * rl, of[9] * rl), pack2(of[10] * rl, of[11] * rl)};
}

struct K2In { const u16 *x, *bre, *bim, *cre, *cim, *dskip; float* out; };

__device__ __forceinline__ f32x2 pkfma(f32x2 a, f32x2 b, f32x2 c) { return __builtin_elementwise_fma(a, b, c); }

__device__ __forceinline__ void s1_role(int sbk, const K2In& in, char* __restrict__ ws, ldsp smem, int tid, int wave, int lane STAMP_ARG) {
  const int c = 2 * (sbk & 7) + (sbk >> 3) / 10, pt = (sbk >> 3) % 10;
  if (wave >= 4) {
    const int lw = wave - 4;
    unsigned aoff[4], boff[4];
    nk_offsets<4>(aoff, lw, lane, [&](int row) {
      const int i = row & 31, t = (row & 64) + 32 * ((i >> 2) & 1) + 16 * ((row >> 5) & 1) + (i & 3) + 4 * (i >> 3);
      return (unsigned)(t * (DM * 2));
    });
    nk_offsets<4>(boff, lw, lane, [&](int row) { return (unsigned)((((row >> 6) << 5) + (row & 31)) * (DM * 2)); });
    const char* bb0 = (const char*)((lw & 1) ? in.bim : in.bre) + (size_t)pt * 64 * (DM * 2);
    gemm_loader<3, 2, 2, 2, 10>(smem, lw, (const char*)in.x + (size_t)c * 128 * (DM * 2), aoff, [&](int s) { return bb0 + (size_t)s * 128; }, boff);
    return;
  }
  const int wm = wave >> 1, wn = wave & 1, r = lane & 31, hh = lane >> 5;
  const int p = pt * 64 + wn * 32 + r;
  const f32x4 t0 = ((const f32x4*)(ws + WS_TAB))[p * 2], t1 = ((const f32x4*)(ws + WS_TAB))[p * 2 + 1];
  const f32x2 lb128 = ((const f32x2*)(ws + WS_TAB))[(pt * 64 + lane) * 4 + 3];
  const cf lam = {t0[0], t0[1]}, c2 = {t0[2], t0[3]}, l32 = {t1[0], t1[1]};
  f32x16 acc[2][2];
  zero_acc(acc);
  gemm_consumer<3, 2, false, 2, 2, false, 10>(acc, smem, wave, lane STAMP_PASS);
  const cf lam2 = cmul(lam, lam);
  const f32x2 cr = {c2.re, c2.re}, ci = {c2.im, c2.im}, nci = {-c2.im, -c2.im};
  const f32x2 l2r = {lam2.re, lam2.re}, l2i = {lam2.im, lam2.im}, nl2i = {-lam2.im, -lam2.im};
  f32x2 er = {0.f, 0.f}, ei = {0.f, 0.f};
#pragma unroll
  for (int mt = 0; mt < 2; ++mt)
#pragma unroll
    for (int j = 0; j < 16; j += 2) {
      const f32x2 br = {acc[mt][0][j], acc[mt][0][j + 1]}, bi = {acc[mt][1][j], acc[mt][1][j + 1]};
      const f32x2 sr = pkfma(nci, bi, cr * br), si = pkfma(ci, br, cr * bi);
      acc[mt][0][j] = sr[0]; acc[mt][0][j + 1] = sr[1]; acc[mt][1][j] = si[0]; acc[mt][1][j + 1] = si[1];
      const f32x2 nr = pkfma(l2r, er, pkfma(nl2i, ei, sr)), ni = pkfma(l2r, ei, pkfma(l2i, er, si));
      er = nr; ei = ni;
    }
  const cf e = cfma(lam, (cf){er[0], ei[0]}, (cf){er[1], ei[1]});
  bar_lds();
  LAS f32x2* ex = (LAS f32x2*)smem;
  LAS f32x2* part = ex + 256;
  const int sub = 2 * wm + hh, pl = wn * 32 + r;
  ex[sub * 64 + pl] = (f32x2){e.re, e.im};
  bar_lds();
  cf es[4];
#pragma unroll
  for (int j = 0; j < 4; ++j) { const f32x2 t = ex[j * 64 + pl]; es[j] = {t[0], t[1]}; }
  gu64* gran = (gu64*)(ws + WS_GRAN);
  if (sub == 0) {
    cf E = es[0];
#pragma unroll
    for (int j = 1; j < 4; ++j) E = cfma(l32, E, es[j]);
    gu64* g = gran + ((size_t)c * 640 + p) * 2;
    __hip_atomic_store(g, (1ull << 32) | __float_as_uint(E.re), RLX_AGENT);
    __hip_atomic_store(g + 1, (1ull << 32) | __float_as_uint(E.im), RLX_AGENT);
  }
  STAMP(4);
  {
    const int pp = pt * 64 + lane;
    const cf L1 = {lb128[0], lb128[1]}, L2 = cmul(L1, L1), L4 = cmul(L2, L2);
    cf a = {0.f, 0.f};
    int last = -1;
    for (int cc = wave; cc < c; cc += 4) {
      gu64* g = gran + ((size_t)cc * 640 + pp) * 2;
      unsigned long long x0, x1;
      for (;;) {
        x0 = __hip_atomic_load(g, RLX_AGENT);
        x1 = __hip_atomic_load(g + 1, RLX_AGENT);
        if (__all((x0 >> 32) == 1ull && (x1 >> 32) == 1ull)) break;
        __builtin_amdgcn_s_sleep(2);
      }
      a = cfma(L4, a, (cf){__uint_as_float((unsigned)x0), __uint_as_float((unsigned)x1)});
      last = cc;
    }
    const int ex3 = c - 1 - last;
    if (last >= 0) {
      if (ex3 & 1) a = cmul(a, L1);
      if (ex3 & 2) a = cmul(a, L2);
    }
    part[wave * 64 + lane] = (f32x2){a.re, a.im};
  }
  bar_lds();
  STAMP(5);
  cf h;
  {
    cf hin = {0.f, 0.f};
#pragma unroll
    for (int j = 0; j < 4; ++j) { const f32x2 t = part[j * 64 + pl]; hin.re += t[0]; hin.im += t[1]; }
    h = hin;
#pragma unroll
    for (int j = 0; j < 3; ++j)
      if (j < sub) h = cfma(l32, h, es[j]);
  }
  bar_lds();
  constexpr int IP = 288;
#pragma unroll
  for (int mt = 0; mt < 2; ++mt) {
    unsigned wr[8], wi[8];
#pragma unroll
    for (int j = 0; j < 16; j += 2) {
      h = cfma(lam, h, (cf){acc[mt][0][j], acc[mt][1][j]});
      const cf h0 = h;
      h = cfma(lam, h, (cf){acc[mt][0][j + 1], acc[mt][1][j + 1]});
      wr[j >> 1] = pack2(h0.re, h.re);
      wi[j >> 1] = pack2(-h0.im, -h.im);
    }
    ldsp d = smem + pl * IP + (sub * 32 + mt * 16) * 2;
    *(LAS u32x4*)(d) = (u32x4){wr[0], wr[1], wr[2], wr[3]};
    *(LAS u32x4*)(d + 16) = (u32x4){wr[4], wr[5], wr[6], wr[7]};
    *(LAS u32x4*)(d + 64 * IP) = (u32x4){wi[0], wi[1], wi[2], wi[3]};
    *(LAS u32x4*)(d + 64 * IP + 16) = (u32x4){wi[4], wi[5], wi[6], wi[7]};
  }
  bar_lds();
  {
    const int g = lane >> 4, q = (lane & 15) >> 2, pq = lane & 3, i16 = lane & 15;
    const __amdgpu_buffer_rsrc_t rs = __builtin_amdgcn_make_buffer_rsrc((void*)(ws + WS_H), 0, SEQ * 2 * DM * 2, 0x00020000);
#pragma unroll
    for (int it = 0; it < 8; ++it) {
      const int item = it * 4 + wave, tb = item & 7, pb = item >> 3;
      ldsp a = smem + (32 * pb + 8 * g + q) * IP + (16 * tb + 4 * pq) * 2;
      const s16x4 lo = ldstr(a), hi = ldstr(a + 4 * IP);
      const s16x8 v = {lo[0], lo[1], lo[2], lo[3], hi[0], hi[1], hi[2], hi[3]};
      __builtin_amdgcn_raw_buffer_store_b128(__builtin_bit_cast(u32x4, v), rs,
                                             ((c * 128 + 16 * tb + i16) * (2 * DM) + (pb >> 1) * DM + pt * 64 + (pb & 1) * 32 + 8 * g) * 2, 0, 16);
    }
    asm volatile("s_waitcnt vmcnt(0)" ::: "memory");
    bar_lds();
    if (tid == 0) __hip_atomic_fetch_add((gu32*)(ws + WS_CNT + c * 256), 1u, RLX_AGENT);
  }
}

__device__ __forceinline__ void s2_role(int sbk, const K2In& in, char* __restrict__ ws, ldsp smem, int tid, int wave, int lane STAMP_ARG) {
  const int rt = 2 * (sbk & 7) + (sbk >> 3) / 5, ct = (sbk >> 3) % 5;
  constexpr int CP = 128 * 4 + 16;
  if (wave >= 4) {
    const int lw = wave - 4, lt = tid - 256;
    const int ec = ct * 128 + (lt & 31) * 4, er = rt * 128 + (lt >> 5);
    u32x2 xv[16];
#pragma unroll
    for (int i = 0; i < 16; ++i) xv[i] = *(const u32x2*)(in.x + (size_t)(er + 8 * i) * DM + ec);
    const u32x2 dv = *(const u32x2*)(in.dskip + ec);
    unsigned aoff[4], boff[4];
    nk_offsets<4>(aoff, lw, lane, [&](int row) { return (unsigned)(row * (2 * DM * 2)); });
    nk_offsets<4>(boff, lw, lane, [&](int row) { return (unsigned)(row * (DM * 2)); });
    const char* bre = (const char*)in.cre + (size_t)ct * 128 * (DM * 2);
    const char* bim = (const char*)in.cim + (size_t)ct * 128 * (DM * 2);
    if (wave == 4) {
      gu32* cnt = (gu32*)(ws + WS_CNT + rt * 256);
      while (__builtin_amdgcn_readfirstlane(__hip_atomic_load(cnt, RLX_AGENT)) < 10u) __builtin_amdgcn_s_sleep(8);
      __builtin_amdgcn_fence(__ATOMIC_ACQUIRE, "agent");
      asm volatile("s_waitcnt vmcnt(0)" ::: "memory");
    }
    bar_lds();
    gemm_loader<3, 2, 2, 2, 20>(smem, lw, ws + WS_H + (size_t)rt * 128 * (2 * DM * 2), aoff,
                             [&](int s) { return (s < 10 ? bre : bim - 10 * 128) + (size_t)s * 128; }, boff);
    bar_lds();
    bar_lds();
    const float d0 = bflo(dv[0]), d1 = bfhi(dv[0]), d2 = bflo(dv[1]), d3 = bfhi(dv[1]);
#pragma unroll
    for (int i = 0; i < 16; ++i) {
      f32x4 y = *(const LAS f32x4*)(smem + ((lt >> 5) + 8 * i) * CP + (lt & 31) * 16);
      y[0] += rbf(bflo(xv[i][0]) * d0);
      y[1] += rbf(bfhi(xv[i][0]) * d1);
      y[2] += rbf(bflo(xv[i][1]) * d2);
      y[3] += rbf(bfhi(xv[i][1]) * d3);
      *(f32x4*)(in.out + (size_t)(er + 8 * i) * (2 * DM) + ec) = y;
    }
  } else {
    const int wm = wave >> 1, wn = wave & 1, r = lane & 31, hh = lane >> 5;
    f32x16 acc[2][2];
    zero_acc(acc);
    bar_lds();
    STAMP(4);
    gemm_consumer<3, 2, false, 2, 2, true, 20>(acc, smem, wave, lane STAMP_PASS);
    bar_lds();
#pragma unroll
    for (int mt = 0; mt < 2; ++mt)
#pragma unroll
      for (int nt = 0; nt < 2; ++nt)
#pragma unroll
        for (int g = 0; g < 4; ++g)
          *(LAS f32x4*)(smem + (wm * 64 + mt * 32 + r) * CP + (wn * 64 + nt * 32 + 8 * g + 4 * hh) * 4) =
              (f32x4){acc[mt][nt][4 * g], acc[mt][nt][4 * g + 1], acc[mt][nt][4 * g + 2], acc[mt][nt][4 * g + 3]};
    bar_lds();
  }
}

constexpr int K2_LDS = 5 * 16384;
#define NS1 160
#define NS2 80
#define S1_PRIO_C 2
#define S1_PRIO_L 2
constexpr int N_ATT = 256, N_S1 = NS1, N_S2 = NS2;
__global__ __launch_bounds__(512, 4) void k2_attn_ssm(K2In in, char* __restrict__ ws) {
  extern __shared__ __attribute__((aligned(16))) char smem_[];
  ldsp smem = (ldsp)smem_;
  const int tid = threadIdx.x, lane = tid & 63, wave = __builtin_amdgcn_readfirstlane(tid >> 6);
  STAMP_DECL STAMP(0);
  const int b = blockIdx.x;
  if (b < N_ATT) attention_role(b, ws, smem, wave, lane STAMP_PASS);
  else {
    if (wave >= 4) __builtin_amdgcn_s_setprio(S1_PRIO_L); else __builtin_amdgcn_s_setprio(S1_PRIO_C);
    if (b < N_ATT + N_S1) s1_role(b - N_ATT, in, ws, smem, tid, wave, lane STAMP_PASS);
    else s2_role(b - N_ATT - N_S1, in, ws, smem, tid, wave, lane STAMP_PASS);
  }
  STAMP_END(ws, 1);
}

constexpr int K3_LDS = 4 * 24576;
__global__ __launch_bounds__(512, 2) void k3_out(const u16* __restrict__ wo, const char* __restrict__ ws, float* __restrict__ out) {
  extern __shared__ __attribute__((aligned(16))) char smem_[];
  ldsp smem = (ldsp)smem_;
  const int tid = threadIdx.x, lane = tid & 63, wave = __builtin_amdgcn_readfirstlane(tid >> 6);
  const int b = blockIdx.x, idx = b >> 3, rt = 4 * (b & 7) + idx / 5, ct = idx % 5;
  STAMP_DECL STAMP(0);
  constexpr int CP = 128 * 4 + 16;
  if (wave >= 4) {
    const int lw = wave - 4;
    unsigned aoff[2], boff[4];
    nk_offsets<2>(aoff, lw, lane, [&](int row) { return (unsigned)(row * (DM * 2)); });
    kn_offsets(boff, lw, lane, DM * 2, 15);
    const char* bb0 = (const char*)wo + ct * 256;
    gemm_loader<4, 4, 1, 2, 10>(smem, lw, ws + WS_O + (size_t)rt * 64 * (DM * 2), aoff, [&](int s) { return bb0 + (size_t)s * (64 * DM * 2); }, boff);
    bar_lds();
  } else {
    const int wm = wave >> 1, wn = wave & 1, r = lane & 31, hh = lane >> 5;
    f32x16 acc[1][2];
    zero_acc(acc);
    gemm_consumer<4, 4, true, 1, 2, true, 10>(acc, smem, wave, lane STAMP_PASS);
    bar_lds();
#pragma unroll
    for (int nt = 0; nt < 2; ++nt)
#pragma unroll
      for (int g = 0; g < 4; ++g)
        *(LAS f32x4*)(smem + (wm * 32 + r) * CP + (wn * 64 + nt * 32 + 8 * g + 4 * hh) * 4) =
            (f32x4){rbf(acc[0][nt][4 * g]), rbf(acc[0][nt][4 * g + 1]), rbf(acc[0][nt][4 * g + 2]), rbf(acc[0][nt][4 * g + 3])};
  }
  bar_lds();
#pragma unroll
  for (int i = 0; i < 4; ++i) {
    const int row = (tid >> 5) + 16 * i;
    const f32x4 y = *(const LAS f32x4*)(smem + row * CP + (tid & 31) * 16);
    *(f32x4*)(out + (size_t)(rt * 64 + row) * (2 * DM) + DM + ct * 128 + (tid & 31) * 4) = y;
  }
  STAMP_END((char*)ws, 2);
}

extern "C" void kernel_launch(void* const* d_in, const int* in_sizes, int n_in, void* d_out, int out_size, void* d_ws,
                              size_t ws_size, hipStream_t stream) {
  const u16* A_x = (const u16*)d_in[0];
  const u16* lre = (const u16*)d_in[1];
  const u16* lim = (const u16*)d_in[2];
  const u16* bre = (const u16*)d_in[3];
  const u16* bim = (const u16*)d_in[4];
  const u16* cre = (const u16*)d_in[5];
  const u16* cim = (const u16*)d_in[6];
  const u16* logdt = (const u16*)d_in[7];
  const u16* dskip = (const u16*)d_in[8];
  const u16* B_x = (const u16*)d_in[9];
  const u16* wqkv = (const u16*)d_in[10];
  const u16* wo = (const u16*)d_in[11];
  char* ws = (char*)d_ws;
  (void)hipFuncSetAttribute((const void*)k1_qkv, hipFuncAttributeMaxDynamicSharedMemorySize, K1_LDS);
  (void)hipFuncSetAttribute((const void*)k2_attn_ssm, hipFuncAttributeMaxDynamicSharedMemorySize, K2_LDS);
  (void)hipFuncSetAttribute((const void*)k3_out, hipFuncAttributeMaxDynamicSharedMemorySize, K3_LDS);
#define ONLY 7
  K1In in1 = {B_x, wqkv, lre, lim, logdt};
  if (ONLY & 1) k1_qkv<<<256, 512, K1_LDS, stream>>>(in1, ws);
  K2In in2 = {A_x, bre, bim, cre, cim, dskip, (float*)d_out};
  if (ONLY & 2) k2_attn_ssm<<<N_ATT + N_S1 + N_S2, 512, K2_LDS, stream>>>(in2, ws);
  if (ONLY & 4) k3_out<<<160, 512, K3_LDS, stream>>>(wo, ws, (float*)d_out);
}
```

```cpp
#include <hip/hip_runtime.h>
#include <hip/hip_bf16.h>
#include <stdint.h>
#include <type_traits>

typedef __attribute__((ext_vector_type(8))) __bf16 bf16x8;
typedef __attribute__((ext_vector_type(4))) short s16x4;
typedef __attribute__((ext_vector_type(8))) short s16x8;
typedef __attribute__((ext_vector_type(16))) float f32x16;
typedef __attribute__((ext_vector_type(4))) float f32x4;
typedef __attribute__((ext_vector_type(2))) float f32x2;
typedef __attribute__((ext_vector_type(4))) unsigned u32x4;
typedef __attribute__((ext_vector_type(2))) unsigned u32x2;
typedef unsigned short u16;
#define LAS __attribute__((address_space(3)))
#define GAS __attribute__((address_space(1)))
typedef LAS char* ldsp;
typedef GAS unsigned long long gu64;
typedef GAS unsigned gu32;

constexpr int SEQ = 2048, DM = 640, NH = 32, DH = 20;
constexpr int VSEG = 352, KOFF = 4 * VSEG, KVREC = KOFF + 1280;
constexpr int QREC = 1280;

constexpr size_t WS_CNT = 0;
constexpr size_t WS_GRAN = 4096;
constexpr size_t WS_ZERO_BYTES = WS_GRAN + 16 * 640 * 2 * 8;
constexpr size_t WS_N2 = 262144;
constexpr size_t WS_TAB = WS_N2 + 16384;
constexpr size_t WS_KV = WS_TAB + 32768;
constexpr size_t WS_Q = WS_KV + (size_t)NH * 64 * KVREC;
constexpr size_t WS_O = WS_Q + (size_t)NH * 64 * QREC;
constexpr int OREC = 48;
constexpr size_t WS_H = WS_O + (size_t)NH * SEQ * OREC;
constexpr size_t WS_END = WS_H + (size_t)SEQ * 2 * DM * 2;

constexpr float QSCALE = 0.22360679774997896f * 1.4426950408889634f;
constexpr float SAFE_BOUND = 40.0f;

#define STAMP_DECL
#define STAMP(i) do {} while (0)
#define STAMP_END(ws, kid) do {} while (0)
#define STAMP_ARG
#define STAMP_PASS

#define SBAR() __builtin_amdgcn_sched_barrier(0)
#define PIN(x) asm volatile("" : "+v"(x))
#define MFMA32(a, b, c) __builtin_amdgcn_mfma_f32_32x32x16_bf16(a, b, c, 0, 0, 0)
#define MFMA8(a, b, c) __builtin_amdgcn_mfma_f32_32x32x8bf16_1k(a, b, c, 0, 0, 0)
#define H_AUX 16
#define OUT_AUX 16
#define REC_AUX 16
#define OUT_NT 0
#define ST_OUT(p, v) __builtin_amdgcn_raw_buffer_store_b128(__builtin_bit_cast(u32x4, v), __builtin_amdgcn_make_buffer_rsrc((void*)out, 0, SEQ * 2 * DM * 4, 0x00020000), (int)(((p) - out) * 4), 0, OUT_AUX)
#define RLX_AGENT __ATOMIC_RELAXED, __HIP_MEMORY_SCOPE_AGENT

__device__ __forceinline__ void glds16(const void* sbase, unsigned voff, unsigned lds_addr) {
  unsigned keep;
  asm volatile("s_mov_b32 %0, m0\n\ts_mov_b32 m0, %3\n\ts_nop 0\n\tglobal_load_lds_dwordx4 %1, %2\n\ts_mov_b32 m0, %0"
               : "=&s"(keep) : "v"(voff), "s"(sbase), "s"(lds_addr) : "memory");
}
__device__ __forceinline__ void warm_pieces(const char* p, int n, int lane, unsigned scratch) {
  for (int i = 0; i < n; ++i) glds16(p + (size_t)i * 1024, lane * 16, scratch);
}
__device__ __forceinline__ void warm_pieces_paced(const char* p, int n, int lane, unsigned scratch) {
  for (int i = 0; i < n; ++i) {
    __builtin_amdgcn_s_sleep(1);
    glds16(p + (size_t)i * 1024, lane * 16, scratch);
  }
}
template <int N> __device__ __forceinline__ void wait_vm_bar() {
  asm volatile("s_waitcnt vmcnt(%0) lgkmcnt(0)\n\ts_barrier" ::"i"(N) : "memory");
}
__device__ __forceinline__ void bar_lds() { asm volatile("s_waitcnt lgkmcnt(0)\n\ts_barrier" ::: "memory"); }
__device__ __forceinline__ float bf2f(u16 v) { return __uint_as_float((unsigned)v << 16); }
__device__ __forceinline__ float bflo(unsigned v) { return __uint_as_float(v << 16); }
__device__ __forceinline__ float bfhi(unsigned v) { return __uint_as_float(v & 0xffff0000u); }
__device__ __forceinline__ unsigned pack2(float lo, float hi) {
  typedef __attribute__((ext_vector_type(2))) __bf16 bf16x2;
  bf16x2 r; r[0] = (__bf16)lo; r[1] = (__bf16)hi;
  return __builtin_bit_cast(unsigned, r);
}
__device__ __forceinline__ float wave_max_nonneg(float v) {
  int x = __float_as_int(v);
  x = max(x, __builtin_amdgcn_update_dpp(0, x, 0x111, 0xf, 0xf, true));
  x = max(x, __builtin_amdgcn_update_dpp(0, x, 0x112, 0xf, 0xf, true));
  x = max(x, __builtin_amdgcn_update_dpp(0, x, 0x114, 0xf, 0xf, true));
  x = max(x, __builtin_amdgcn_update_dpp(0, x, 0x118, 0xf, 0xf, true));
  x = max(x, __builtin_amdgcn_update_dpp(0, x, 0x142, 0xa, 0xf, true));
  x = max(x, __builtin_amdgcn_update_dpp(0, x, 0x143, 0xc, 0xf, true));
  return __int_as_float(x);
}
__device__ __forceinline__ float rbf(float v) { return (float)(__bf16)v; }
__device__ __forceinline__ bf16x8 ldsA(ldsp p) { return *(const LAS bf16x8*)p; }
__device__ __forceinline__ s16x4 ldstr(ldsp p) {
  typedef short v4i16_t __attribute__((ext_vector_type(4)));
  return __builtin_bit_cast(s16x4, __builtin_amdgcn_ds_read_tr16_b64_v4i16((LAS v4i16_t*)p));
}

__device__ __forceinline__ int swz64(int row) { return (row >> 1) & 7; }

template <int MT, int NT, int NSA, int NSB> struct GemmGeo {
  static constexpr int ASZ = 64 * MT * 128, BSZ = 64 * NT * 128, BOFF = NSA * ASZ, BYTES = BOFF + NSB * BSZ;
  static constexpr int NA = 2 * MT, NB = 2 * NT;
};

template <int DA, int DB, int NA, int NB, int NK>
constexpr int pieces_after(int kt) {
  int n = 0;
  bool seenA = false, seenB = false;
  for (int s = 0; s < DA; ++s) {
    if (seenA && seenB) n += NA;
    if (s == kt) seenA = true;
    if (s < DB) { if (seenA && seenB) n += NB; if (s == kt) seenB = true; }
  }
  for (int j = 0; j < kt; ++j) {
    if (j + DB < NK) { if (seenA && seenB) n += NB; if (j + DB == kt) seenB = true; }
    if (j + DA < NK) { if (seenA && seenB) n += NA; if (j + DA == kt) seenA = true; }
  }
  return n;
}
template <int I, int N, class F> __device__ __forceinline__ void static_for(F f) {
  if constexpr (I < N) { f(std::integral_constant<int, I>{}); static_for<I + 1, N>(f); }
}

template <int NSA, int NSB, int MT, int NT, int NK, class BBase>
__device__ __forceinline__ void gemm_loader(ldsp smem, int lw, const char* abase, const unsigned (&aoff)[2 * MT], BBase bbase,
                                            const unsigned (&boff)[2 * NT]) {
  typedef GemmGeo<MT, NT, NSA, NSB> G;
  static_assert(NSB <= NSA && NSB >= 2, "ring shapes");
  constexpr int DA = NSA - 1, DB = NSB - 1, NA = G::NA, NB = G::NB;
  const unsigned lds0 = (unsigned)(uintptr_t)smem;
  auto issueA = [&](int stage) {
    const char* ab = abase + (size_t)stage * 128;
#pragma unroll
    for (int i = 0; i < NA; ++i) glds16(ab, aoff[i], lds0 + (stage % NSA) * G::ASZ + (lw * NA + i) * 1024);
  };
  auto issueB = [&](int stage) {
    const char* bb = bbase(stage);
#pragma unroll
    for (int i = 0; i < NB; ++i) glds16(bb, boff[i], lds0 + G::BOFF + (stage % NSB) * G::BSZ + (lw * NB + i) * 1024);
  };
#pragma unroll
  for (int s = 0; s < DA; ++s) {
    issueA(s);
    if (s < DB) issueB(s);
  }
  static_for<0, NK>([&](auto ic) {
    constexpr int kt = decltype(ic)::value;
    wait_vm_bar<pieces_after<DA, DB, NA, NB, NK>(kt)>();
    if constexpr (kt + DB < NK) issueB(kt + DB);
    if constexpr (kt + DA < NK) issueA(kt + DA);
  });
}

template <int NSA, int NSB, bool BKN, int MT, int NT, bool SWAP, int NK, int PACE = 0>
__device__ __forceinline__ void gemm_consumer(f32x16 (&acc)[MT][NT], ldsp smem, int wave, int lane STAMP_ARG) {
  typedef GemmGeo<MT, NT, NSA, NSB> G;
  static_assert(!BKN || NT == 2, "transposed B image is 128 columns wide");
  const int wm = wave >> 1, wn = wave & 1, r = lane & 31, hh = lane >> 5;
  unsigned fa[4], fb[BKN ? NT : 1];
#pragma unroll
  for (int ks = 0; ks < 4; ++ks) fa[ks] = r * 128 + (((2 * ks + hh) ^ swz64(r)) << 4);
  if constexpr (BKN) {
    const int nh = (lane >> 4) & 1, q = (lane & 15) >> 2, p = lane & 3;
#pragma unroll
    for (int nt = 0; nt < NT; ++nt)
      fb[nt] = (8 * hh + q) * 256 + (((((2 * wn + nt) ^ q) << 2) + 2 * nh + (p >> 1)) << 4) + (p & 1) * 8;
  } else fb[0] = 0;
  bf16x8 fA[2][MT], fB[2][NT];
  auto loadf = [&](int set, ldsp pa, ldsp pb, int ks) {
#pragma unroll
    for (int mt = 0; mt < MT; ++mt) fA[set][mt] = ldsA(pa + fa[ks] + (wm * 32 * MT + mt * 32) * 128);
#pragma unroll
    for (int nt = 0; nt < NT; ++nt) {
      if constexpr (BKN) {
        const s16x4 lo = ldstr(pb + fb[nt] + ks * 16 * 256), hi = ldstr(pb + fb[nt] + (ks * 16 + 4) * 256);
        const s16x8 v = {lo[0], lo[1], lo[2], lo[3], hi[0], hi[1], hi[2], hi[3]};
        fB[set][nt] = __builtin_bit_cast(bf16x8, v);
      } else {
        fB[set][nt] = ldsA(pb + fa[ks] + (wn * 32 * NT + nt * 32) * 128);
      }
    }
  };
  STAMP(1);
  bar_lds();
  STAMP(2);
  loadf(0, smem, smem + G::BOFF, 0);
  int sa = 0, sb = 0;
  for (int kt = 0; kt < NK; ++kt) {
    ldsp pa = smem + sa * G::ASZ, pb = smem + G::BOFF + sb * G::BSZ;
    sa = (sa + 1 == NSA) ? 0 : sa + 1;
    sb = (sb + 1 == NSB) ? 0 : sb + 1;
#pragma unroll
    for (int ks = 0; ks < 4; ++ks) {
      if (ks < 3) {
        loadf((ks + 1) & 1, pa, pb, ks + 1);
      } else if (kt + 1 < NK) {
        bar_lds();
        loadf(0, smem + sa * G::ASZ, smem + G::BOFF + sb * G::BSZ, 0);
      }
      SBAR();
#pragma unroll
      for (int mt = 0; mt < MT; ++mt)
#pragma unroll
        for (int nt = 0; nt < NT; ++nt) {
          acc[mt][nt] = SWAP ? MFMA32(fB[ks & 1][nt], fA[ks & 1][mt], acc[mt][nt]) : MFMA32(fA[ks & 1][mt], fB[ks & 1][nt], acc[mt][nt]);
          if constexpr (PACE == 1) { SBAR(); __builtin_amdgcn_s_sleep(1); SBAR(); }
          if constexpr (PACE == 2) { SBAR(); __builtin_amdgcn_s_sleep(2); SBAR(); }
          if constexpr (PACE == 3) { SBAR(); __builtin_amdgcn_s_sleep(1); if (nt == NT - 1) __builtin_amdgcn_s_sleep(1); SBAR(); }
        }
      SBAR();
    }
  }
  STAMP(3);
}

template <int N, class RowFn>
__device__ __forceinline__ void nk_offsets(unsigned (&off)[N], int lw, int lane, RowFn rowfn) {
#pragma unroll
  for (int i = 0; i < N; ++i) {
    const int row = (lw * N + i) * 8 + (lane >> 3);
    off[i] = rowfn(row) + (((lane & 7) ^ swz64(row)) << 4);
  }
}
__device__ __forceinline__ void kn_offsets(unsigned (&off)[4], int lw, int lane, unsigned pitch, int cmax) {
#pragma unroll
  for (int i = 0; i < 4; ++i) {
    const int row = (lw * 4 + i) * 4 + (lane >> 4);
    const int c = min((lane & 15) ^ ((row & 3) << 2), cmax);
    off[i] = row * pitch + c * 16;
  }
}
template <int MT, int NT> __device__ __forceinline__ void zero_acc(f32x16 (&acc)[MT][NT]) {
#pragma unroll
  for (int i = 0; i < MT; ++i)
#pragma unroll
    for (int j = 0; j < NT; ++j) acc[i][j] = (f32x16){};
}

#define K1_MAP 0
#define K1_NS 4
constexpr int K1_LDS = K1_NS * 32768;
struct cf { float re, im; };
__device__ __forceinline__ cf cmul(cf a, cf b) { return {a.re * b.re - a.im * b.im, a.re * b.im + a.im * b.re}; }
__device__ __forceinline__ cf cfma(cf a, cf b, cf c) {
  return {fmaf(a.re, b.re, fmaf(-a.im, b.im, c.re)), fmaf(a.re, b.im, fmaf(a.im, b.re, c.im))};
}
struct K1In { const u16 *x, *w, *lre, *lim, *logdt, *ax, *bre, *bim; };
__global__ __launch_bounds__(512, 2) void k1_qkv(K1In in, char* __restrict__ ws) {
  const u16* __restrict__ x = in.x;
  const u16* __restrict__ w = in.w;
  extern __shared__ __attribute__((aligned(16))) char smem_[];
  ldsp smem = (ldsp)smem_;
  const int tid = threadIdx.x, lane = tid & 63, wave = __builtin_amdgcn_readfirstlane(tid >> 6);
  STAMP_DECL STAMP(0);
  const int b = blockIdx.x, xcd = b & 7, idx = b >> 3;
  const int rt = 4 * (xcd & 3) + (idx & 3), ct = 8 * (xcd >> 2) + (idx >> 2);
  constexpr int CP = 264;
  if (wave >= 4) {
    unsigned aoff[4], boff[4];
    nk_offsets<4>(aoff, wave - 4, lane, [&](int row) { return (unsigned)(row * (DM * 2)); });
    kn_offsets(boff, wave - 4, lane, 3 * DM * 2, 14);
    const char* bb0 = (const char*)w + ct * 240;
    gemm_loader<K1_NS, K1_NS, 2, 2, 10>(smem, wave - 4, (const char*)x + (size_t)rt * 128 * (DM * 2), aoff,
                             [&](int s) { return bb0 + (size_t)s * (64 * 3 * DM * 2); }, boff);
    {
      const int pi = b * 4 + (wave - 4);
      const unsigned sc = __builtin_amdgcn_readfirstlane((unsigned)(uintptr_t)smem + K1_NS * 16384 + (8 % K1_NS) * 16384);
      if (pi < 640) warm_pieces((const char*)in.ax + (size_t)pi * 4096, 4, lane, sc);
      else if (pi < 840) warm_pieces((const char*)in.bre + (size_t)(pi - 640) * 4096, 4, lane, sc);
      else if (pi < 1024) warm_pieces((const char*)in.bim + (size_t)(pi - 840) * 4096, 4, lane, sc);
      if (pi < 16) warm_pieces((const char*)in.bim + (size_t)(184 + pi) * 4096, 4, lane, sc);
    }
    bar_lds();
  } else {
    if (tid < 41) *(u32x4*)(ws + (size_t)b * 656 + tid * 16) = (u32x4){0u, 0u, 0u, 0u};
    if (b < 10 && wave == 0) {
      const int p = b * 64 + lane;
      const float lr = bf2f(in.lre[p]), li = bf2f(in.lim[p]);
      const float dt = rbf(expf(bf2f(in.logdt[p])));
      const float ea = expf(dt * lr);
      float sn, cs;
      sincosf(dt * li, &sn, &cs);
      const cf lam = {ea * cs, ea * sn};
      const float nr = lam.re - 1.0f, ni = lam.im, den = lr * lr + li * li;
      cf l32 = lam;
#pragma unroll
      for (int i = 0; i < 5; ++i) l32 = cmul(l32, l32);
      const cf l64 = cmul(l32, l32), l128 = cmul(l64, l64);
      f32x4* t = (f32x4*)(ws + WS_TAB) + p * 2;
      t[0] = (f32x4){lam.re, lam.im, 2.f * (nr * lr + ni * li) / den, 2.f * (ni * lr - nr * li) / den};
      t[1] = (f32x4){l32.re, l32.im, l128.re, l128.im};
    }
    f32x16 acc[2][2];
    zero_acc(acc);
    gemm_consumer<K1_NS, K1_NS, true, 2, 2, true, 10>(acc, smem, wave, lane STAMP_PASS);
    bar_lds();
    const int wm = wave >> 1, wn = wave & 1, r = lane & 31, hh = lane >> 5;
#pragma unroll
    for (int mt = 0; mt < 2; ++mt)
#pragma unroll
      for (int nt = 0; nt < 2; ++nt)
#pragma unroll
        for (int g = 0; g < 4; ++g) {
          const u32x2 v = {pack2(acc[mt][nt][4 * g], acc[mt][nt][4 * g + 1]), pack2(acc[mt][nt][4 * g + 2], acc[mt][nt][4 * g + 3])};
          *(LAS u32x2*)(smem + (wm * 64 + mt * 32 + r) * CP + (wn * 64 + nt * 32 + 8 * g + 4 * hh) * 2) = v;
        }
  }
  bar_lds();
  STAMP(4);
  float* n2 = (float*)(ws + WS_N2);
  const __amdgpu_buffer_rsrc_t rrec = __builtin_amdgcn_make_buffer_rsrc((void*)(ws + WS_KV), 0, (int)(WS_O - WS_KV), 0x00020000);
  {
    const int row = tid & 127, grow = rt * 128 + row, t32 = grow >> 5, rr = grow & 31;
    unsigned v[2][10];
#pragma unroll
    for (int i = 0; i < 2; ++i) {
      const int part = min((wave >> 1) + 4 * i, 5);
#pragma unroll
      for (int j = 0; j < 5; ++j) {
        const u32x2 t = *(const LAS u32x2*)(smem + row * CP + part * 40 + j * 8);
        v[i][2 * j] = t[0]; v[i][2 * j + 1] = t[1];
      }
    }
#pragma unroll
    for (int i = 0; i < 2; ++i) {
      const int part = (wave >> 1) + 4 * i, gp = 6 * ct + part, type = gp >> 5, head = gp & 31;
      if (part < 6 && type < 2) {
        if (type == 0) {
#pragma unroll
          for (int j = 0; j < 10; ++j) v[i][j] = pack2(bflo(v[i][j]) * QSCALE, bfhi(v[i][j]) * QSCALE);
        }
        const int rec = type == 0 ? (int)(WS_Q - WS_KV) + (head * 64 + t32) * QREC : (head * 64 + t32) * KVREC + KOFF;
        __builtin_amdgcn_raw_buffer_store_b128((u32x4){v[i][0], v[i][1], v[i][2], v[i][3]}, rrec, rec + rr * 16, 0, REC_AUX);
        __builtin_amdgcn_raw_buffer_store_b128((u32x4){v[i][4], v[i][5], v[i][6], v[i][7]}, rrec, rec + 512 + rr * 16, 0, REC_AUX);
        __builtin_amdgcn_raw_buffer_store_b64((u32x2){v[i][8], v[i][9]}, rrec, rec + 1024 + rr * 8, 0, REC_AUX);
        float ss = 0.f, s2 = 0.f;
#pragma unroll
        for (int j = 0; j < 10; ++j) { const float a = bflo(v[i][j]), c = bfhi(v[i][j]); ss = fmaf(a, a, ss); s2 = fmaf(c, c, s2); }
        ss = wave_max_nonneg(ss + s2);
        if (lane == 63) n2[(type * 32 + head) * 32 + (grow >> 6)] = ss;
      }
    }
  }
  STAMP(5);
  if (6 * ct + 5 >= 64) {
    const int q = (lane & 15) >> 2, p = lane & 3, i16 = lane & 15, grp = tid >> 4;
#pragma unroll
    for (int it = 0; it < 4; ++it) {
      const int item = it * 32 + grp;
      const int cg = item & 7, kt4 = (item >> 3) & 3, sh = item >> 5, s = sh >> 1, h2 = sh & 1;
      const int row0 = 32 * kt4 + 16 * s + 4 * h2 + q;
      ldsp a = smem + row0 * CP + (16 * cg + 4 * p) * 2;
      const s16x4 lo = ldstr(a), hi = ldstr(a + 8 * CP);
      const int c = 16 * cg + i16, part = c / 20, d = c - part * 20, gp = 6 * ct + part;
      if (c < 120 && gp >= 64) {
        const int rec = ((gp - 64) * 64 + rt * 4 + kt4) * KVREC + sh * VSEG + d * 16;
        const s16x8 vv = {lo[0], lo[1], lo[2], lo[3], hi[0], hi[1], hi[2], hi[3]};
        __builtin_amdgcn_raw_buffer_store_b128(__builtin_bit_cast(u32x4, vv), rrec, rec, 0, REC_AUX);
        if (d == 19) __builtin_amdgcn_raw_buffer_store_b128((u32x4){0x3f803f80u, 0x3f803f80u, 0x3f803f80u, 0x3f803f80u}, rrec, rec + 16, 0, REC_AUX);
        if (d == 18) __builtin_amdgcn_raw_buffer_store_b128((u32x4){0u, 0u, 0u, 0u}, rrec, rec + 48, 0, REC_AUX);
      }
    }
  }
  STAMP_END(ws, 0);
}

#define ATT_PRIO 3
#define EXP_W 0
#define ATT_FLIP 6
#define ATT_DMA_T 0
constexpr int ATT_HALF = 4 * KVREC;
constexpr int ATT_STG = 2 * ATT_HALF;
constexpr int ATT_NSTG = 8;

struct AttDma {
  const char* kv; unsigned lds0, vlin, vmid;
  static constexpr int DELTA = 32 * KVREC - ATT_HALF;
  __device__ __forceinline__ void piece(int stage, int buf, int pc) const {
    glds16(kv + (size_t)stage * ATT_HALF + pc * 1024 + (pc > 10 ? DELTA : 0), pc == 10 ? vmid : vlin,
           __builtin_amdgcn_readfirstlane(lds0 + buf * ATT_STG + pc * 1024));
  }
  __device__ __forceinline__ void warm(int stage, int wave) const {
#pragma unroll
    for (int i = 0; i < 3; ++i) {
      const int pc = wave + 8 * i;
      if (pc < 21) glds16(kv + (size_t)stage * ATT_HALF + pc * 1024 + (pc > 10 ? DELTA : 0), pc == 10 ? vmid : vlin,
                          __builtin_amdgcn_readfirstlane(lds0 + 3 * ATT_STG + 3072));
    }
  }
  unsigned v3[3], pc3;
  __device__ __forceinline__ void setup3(int wave) {
    const int p0 = 3 * min(wave, 6);
    pc3 = (p0 + 1) * 1024;
#pragma unroll
    for (int i = 0; i < 3; ++i) v3[i] = p0 + i == 10 ? vmid : (p0 + i > 10 ? vlin + DELTA : vlin);
  }
  __device__ __forceinline__ void three(int stage, int buf) const {
    const char* base = kv + (size_t)stage * ATT_HALF + pc3;
    const unsigned dst = __builtin_amdgcn_readfirstlane(lds0 + buf * ATT_STG + pc3);
    unsigned keep;
    asm volatile("s_mov_b32 %0, m0\n\ts_mov_b32 m0, %5\n\ts_nop 0\n\t"
                 "global_load_lds_dwordx4 %1, %4 offset:-1024\n\tglobal_load_lds_dwordx4 %2, %4\n\t"
                 "global_load_lds_dwordx4 %3, %4 offset:1024\n\ts_mov_b32 m0, %0"
                 : "=&s"(keep) : "v"(v3[0]), "v"(v3[1]), "v"(v3[2]), "s"(base), "s"(dst) : "memory");
  }
};

__device__ __forceinline__ void attention_role(int ab, char* __restrict__ ws, ldsp smem, int wave, int lane STAMP_ARG) {
  const int xg = ab & 7, idx = ab >> 3, head = 4 * xg + (idx & 3), qt = idx >> 2;
  const int r = lane & 31, hh = lane >> 5, qg = wave & 3, kh = wave >> 2;
  AttDma dma;
  dma.kv = ws + WS_KV + (size_t)head * 64 * KVREC;
  dma.lds0 = (unsigned)(uintptr_t)smem;
  dma.vlin = lane * 16;
  dma.vmid = lane * 16 + (lane >= 32 ? AttDma::DELTA : 0);
  dma.setup3(wave);
  dma.three(0, 0);
  const char* qrec = ws + WS_Q + ((size_t)head * 64 + qt * 8 + qg * 2) * QREC;
  const bf16x8 qm0 = *(const bf16x8*)(qrec + lane * 16), qm1 = *(const bf16x8*)(qrec + QREC + lane * 16);
  s16x4 qt0 = *(const s16x4*)(qrec + 1024 + r * 8), qt1 = *(const s16x4*)(qrec + QREC + 1024 + r * 8);
  if (hh) { qt0 = (s16x4){0, 0, 0, 0}; qt1 = qt0; }
  const float* n2 = (const float*)(ws + WS_N2);
  const float kmax = __int_as_float(__builtin_amdgcn_readlane(__float_as_int(wave_max_nonneg(n2[(32 + head) * 32 + r])), 63));
  const float qmax = n2[head * 32 + qt * 4 + qg];
  LAS int* flag = (LAS int*)(smem + 3 * ATT_STG);
  if (lane == 0) flag[wave] = qmax * kmax <= SAFE_BOUND * SAFE_BOUND ? 0 : 1;
  const unsigned hb = kh * ATT_HALF;
  const unsigned ka = hb + KOFF + lane * 16, va = hb + hh * VSEG + min(r, 21) * 16;
  unsigned kta = hb + (hh ? 21 * 16 : KOFF + 1024 + r * 8);
  wait_vm_bar<0>();
  dma.three(1, 1);
  if (qt >= 2) dma.warm(qt, wave);
  const bool slow = __any(flag[lane & 7] != 0) != 0;
  if (slow) {
    float m0 = -INFINITY, m1 = -INFINITY;
    for (int s = 0; s < ATT_NSTG; ++s) {
      ldsp sb = smem + (s & 1) * ATT_STG;
#pragma unroll 1
      for (int t = 0; t < 4; ++t) {
        const bf16x8 k = ldsA(sb + t * KVREC + ka);
        const s16x4 kt = *(const LAS s16x4*)(sb + t * KVREC + kta);
        f32x16 sc = MFMA32(k, qm0, (f32x16){});
        sc = MFMA8(kt, qt0, sc);
#pragma unroll
        for (int j = 0; j < 16; ++j) m0 = fmaxf(m0, sc[j]);
        sc = MFMA32(k, qm1, (f32x16){});
        sc = MFMA8(kt, qt1, sc);
#pragma unroll
        for (int j = 0; j < 16; ++j) m1 = fmaxf(m1, sc[j]);
      }
      bar_lds();
      dma.three((s + 2) & (ATT_NSTG - 1), s & 1);
      if (s + 1 < ATT_NSTG) wait_vm_bar<3>(); else wait_vm_bar<0>();
    }
    m0 = fmaxf(m0, __shfl_xor(m0, 32));
    m1 = fmaxf(m1, __shfl_xor(m1, 32));
    LAS f32x2* mx = (LAS f32x2*)(smem + 3 * ATT_STG + 64);
    if (hh == 0) mx[wave * 32 + r] = (f32x2){m0, m1};
    bar_lds();
    const f32x2 om = mx[(wave ^ 4) * 32 + r];
    m0 = fmaxf(m0, om[0]);
    m1 = fmaxf(m1, om[1]);
    if (hh) {
      const float h0 = rbf(m0), h1 = rbf(m1);
      const unsigned w0 = pack2(-h0, h0 - m0), w1 = pack2(-h1, h1 - m1);
      qt0 = (s16x4){(short)(w0 & 0xffff), (short)(w0 >> 16), 0, 0};
      qt1 = (s16x4){(short)(w1 & 0xffff), (short)(w1 >> 16), 0, 0};
      kta = hb + 20 * 16;
    }
  }
  f32x16 o0, o1;
#pragma unroll
  for (int j = 0; j < 16; ++j) { o0[j] = 0.f; o1[j] = 0.f; }
  STAMP(2);
  f32x16 sA, sB;
  bf16x8 kf, v0, v1;
  s16x4 kt;
  u32x4 p0, p1 = {0u, 0u, 0u, 0u};
  const f32x16 zero = {};
  kf = ldsA(smem + ka); kt = *(const LAS s16x4*)(smem + kta);
  v0 = ldsA(smem + va);
  v1 = v0;
  sA = MFMA32(kf, qm0, zero);
  sA = MFMA8(kt, qt0, sA);
#define ATT_BAR() wait_vm_bar<0>()
#define ATT_DMA_ON 1
#define DSTAMP(i) do {} while (0)
#define EX(x) __builtin_amdgcn_exp2f(x)
#define LDK(base, J) do { kf = ldsA((base) + (J) * KVREC + ka); kt = *(const LAS s16x4*)((base) + (J) * KVREC + kta); } while (0)
#define ATT_STEP(SC, SN, QM, QT, OP, OC, T)                                                             \
  {                                                                                                     \
    float e0, e1, e2, e3, e4, e5, e6, e7;                                                               \
            \
    if (ATT_PRIO == 3) { if ((T) == 0) { if (kh) __builtin_amdgcn_s_setprio(1); }                       \
                         else if ((T) == ATT_FLIP) { if (kh) __builtin_amdgcn_s_setprio(0); } }         \
    e0 = EX(SC[0]); e1 = EX(SC[1]);                                                                     \
    PIN(e0); PIN(e1); SBAR();                                                                           \
    OP = MFMA32(v1, __builtin_bit_cast(bf16x8, p1), OP);                                                \
    if (((T) & 1) == 0) v1 = ldsA(sb + ((T) >> 1) * KVREC + va + 2 * VSEG);                             \
    e2 = EX(SC[2]); e3 = EX(SC[3]);                                                                     \
    PIN(e2); PIN(e3); SBAR();                                                                           \
                                      \
    if ((T) == ATT_DMA_T && ATT_DMA_ON) dma.three(min(s + 2, ATT_NSTG - 1), bf2);    \
    SN = MFMA32(kf, QM, zero);                                                                          \
    e4 = EX(SC[4]); e5 = EX(SC[5]); p0[0] = pack2(e0, e1); e6 = EX(SC[6]); e7 = EX(SC[7]);              \
    p0[1] = pack2(e2, e3);                                                                              \
    PIN(e4); PIN(e5); PIN(e6); PIN(e7); PIN(p0); SBAR();                                                \
    SN = MFMA8(kt, QT, SN);                                                                             \
    p0[2] = pack2(e4, e5); e0 = EX(SC[8]); p0[3] = pack2(e6, e7); e1 = EX(SC[9]);                       \
    e2 = EX(SC[10]); e3 = EX(SC[11]);                                                                   \
    PIN(e0); PIN(e1); PIN(e2); PIN(e3); PIN(p0); SBAR();                                                \
    if ((T) == 6) {                                                                                     \
      DSTAMP(3);                                                                                        \
                         \
      if (ATT_DMA_ON) wait_vm_bar<3>(); else ATT_BAR();                                                 \
      DSTAMP(5);                                                                                        \
      LDK(sn, 0);                                                                                       \
    } else if (((T) & 1) == 0) LDK(sb, ((T) >> 1) + 1);                                                 \
    SBAR();                                                                                             \
    OC = MFMA32(v0, __builtin_bit_cast(bf16x8, p0), OC);                                                \
    if ((T) == 7) v0 = ldsA(sn + va);                                                                   \
    else if ((T) & 1) v0 = ldsA(sb + (((T) >> 1) + 1) * KVREC + va);                                    \
    e4 = EX(SC[12]); e5 = EX(SC[13]); e6 = EX(SC[14]); e7 = EX(SC[15]);                                 \
    p1[0] = pack2(e0, e1); p1[1] = pack2(e2, e3); p1[2] = pack2(e4, e5); p1[3] = pack2(e6, e7);         \
    PIN(p1); SBAR();                                                                                    \
  }
  int bf0 = 0;
  for (int s = 0; s < ATT_NSTG; ++s) {
    const int bf1 = bf0 == 2 ? 0 : bf0 + 1, bf2 = bf1 == 2 ? 0 : bf1 + 1;
    ldsp sb = smem + bf0 * ATT_STG;
    ldsp sn = smem + bf1 * ATT_STG;
    bf0 = bf1;
    ATT_STEP(sA, sB, qm1, qt1, o1, o0, 0)
    ATT_STEP(sB, sA, qm0, qt0, o0, o1, 1)
    ATT_STEP(sA, sB, qm1, qt1, o1, o0, 2)
    ATT_STEP(sB, sA, qm0, qt0, o0, o1, 3)
    ATT_STEP(sA, sB, qm1, qt1, o1, o0, 4)
    ATT_STEP(sB, sA, qm0, qt0, o0, o1, 5)
    ATT_STEP(sA, sB, qm1, qt1, o1, o0, 6)
    ATT_STEP(sB, sA, qm0, qt0, o0, o1, 7)
  }
  o1 = MFMA32(v1, __builtin_bit_cast(bf16x8, p1), o1);
#undef ATT_STEP
#undef LDK
#undef EX
  STAMP(3);
  wait_vm_bar<0>();
  LAS float* xch = (LAS float*)smem;
  if (kh) {
#pragma unroll
    for (int j = 0; j < 12; ++j) { xch[((qg * 2 + 0) * 12 + j) * 64 + lane] = o0[j]; xch[((qg * 2 + 1) * 12 + j) * 64 + lane] = o1[j]; }
  }
  bar_lds();
  if (kh == 0) {
    ldsp img = smem + 32768 + qg * (64 * OREC);
#pragma unroll
    for (int blk = 0; blk < 2; ++blk) {
      float of[12];
#pragma unroll
      for (int j = 0; j < 12; ++j) of[j] = (blk ? o1[j] : o0[j]) + xch[((qg * 2 + blk) * 12 + j) * 64 + lane];
      const float rl = 1.0f / __shfl(of[8], r + 32);
      ldsp row = img + (blk * 32 + r) * OREC + hh * 8;
      *(LAS u32x2*)(row) = (u32x2){pack2(of[0] * rl, of[1] * rl), pack2(of[2] * rl, of[3] * rl)};
      *(LAS u32x2*)(row + 16) = (u32x2){pack2(of[4] * rl, of[5] * rl), pack2(of[6] * rl, of[7] * rl)};
      *(LAS u32x2*)(row + 32) = hh ? (u32x2){0u, 0u} : (u32x2){pack2(of[8] * rl, of[9] * rl), pack2(of[10] * rl, of[11] * rl)};
    }
    asm volatile("s_waitcnt lgkmcnt(0)" ::: "memory");
    const __amdgpu_buffer_rsrc_t rs = __builtin_amdgcn_make_buffer_rsrc((void*)(ws + WS_O), 0, NH * SEQ * OREC, 0x00020000);
    const int ob = (head * SEQ + qt * 256 + qg * 64) * OREC;
#pragma unroll
    for (int i = 0; i < 3; ++i)
      __builtin_amdgcn_raw_buffer_store_b128(*(const LAS u32x4*)(img + (i * 64 + lane) * 16), rs, ob + (i * 64 + lane) * 16, 0, 16);
  }
}

#define S1_PACE 1
struct K2In { const u16 *x, *bre, *bim, *cre, *cim, *wo; };
#define WARM_C_DELAY 1
constexpr int K2_SCRATCH = 79 * 1024;

__device__ __forceinline__ f32x2 pkfma(f32x2 a, f32x2 b, f32x2 c) { return __builtin_elementwise_fma(a, b, c); }

__device__ __forceinline__ void s1_role(int sbk, const K2In& in, char* __restrict__ ws, ldsp smem, int tid, int wave, int lane STAMP_ARG) {
  const int c = 2 * (sbk & 7) + (sbk >> 3) / 10, pt = (sbk >> 3) % 10;
  if (wave >= 4) {
    const int lw = wave - 4;
    unsigned aoff[4], boff[4];
    nk_offsets<4>(aoff, lw, lane, [&](int row) {
      const int i = row & 31, t = (row & 64) + 32 * ((i >> 2) & 1) + 16 * ((row >> 5) & 1) + (i & 3) + 4 * (i >> 3);
      return (unsigned)(t * (DM * 2));
    });
    nk_offsets<4>(boff, lw, lane, [&](int row) { return (unsigned)((((row >> 6) << 5) + (row & 31)) * (DM * 2)); });
    const char* bb0 = (const char*)((lw & 1) ? in.bim : in.bre) + (size_t)pt * 64 * (DM * 2);
    gemm_loader<3, 2, 2, 2, 10>(smem, lw, (const char*)in.x + (size_t)c * 128 * (DM * 2), aoff, [&](int s) { return bb0 + (size_t)s * 128; }, boff);
    bar_lds();
    bar_lds();
    {
      const int w80 = (sbk >> 3) * 4 + lw;
      const unsigned sc = __builtin_amdgcn_readfirstlane((unsigned)(uintptr_t)smem + 79 * 1024);
      warm_pieces_paced((const char*)in.cre + (size_t)w80 * 10240, 10, lane, sc);
      warm_pieces_paced((const char*)in.cim + (size_t)w80 * 10240, 10, lane, sc);
      warm_pieces_paced((const char*)in.wo + (size_t)w80 * 10240, 10, lane, sc);
    }
    return;
  }
  const int wm = wave >> 1, wn = wave & 1, r = lane & 31, hh = lane >> 5;
  const int p = pt * 64 + wn * 32 + r;
  const f32x4 t0 = ((const f32x4*)(ws + WS_TAB))[p * 2], t1 = ((const f32x4*)(ws + WS_TAB))[p * 2 + 1];
  const f32x2 lb128 = ((const f32x2*)(ws + WS_TAB))[(pt * 64 + lane) * 4 + 3];
  const cf lam = {t0[0], t0[1]}, c2 = {t0[2], t0[3]}, l32 = {t1[0], t1[1]};
  f32x16 acc[2][2];
  zero_acc(acc);
  gemm_consumer<3, 2, false, 2, 2, false, 10, S1_PACE>(acc, smem, wave, lane STAMP_PASS);
  const cf lam2 = cmul(lam, lam);
  const f32x2 cr = {c2.re, c2.re}, ci = {c2.im, c2.im}, nci = {-c2.im, -c2.im};
  const f32x2 l2r = {lam2.re, lam2.re}, l2i = {lam2.im, lam2.im}, nl2i = {-lam2.im, -lam2.im};
  f32x2 er = {0.f, 0.f}, ei = {0.f, 0.f};
#pragma unroll
  for (int mt = 0; mt < 2; ++mt)
#pragma unroll
    for (int j = 0; j < 16; j += 2) {
      const f32x2 br = {acc[mt][0][j], acc[mt][0][j + 1]}, bi = {acc[mt][1][j], acc[mt][1][j + 1]};
      const f32x2 sr = pkfma(nci, bi, cr * br), si = pkfma(ci, br, cr * bi);
      acc[mt][0][j] = sr[0]; acc[mt][0][j + 1] = sr[1]; acc[mt][1][j] = si[0]; acc[mt][1][j + 1] = si[1];
      const f32x2 nr = pkfma(l2r, er, pkfma(nl2i, ei, sr)), ni = pkfma(l2r, ei, pkfma(l2i, er, si));
      er = nr; ei = ni;
    }
  const cf e = cfma(lam, (cf){er[0], ei[0]}, (cf){er[1], ei[1]});
  bar_lds();
  LAS f32x2* ex = (LAS f32x2*)smem;
  LAS f32x2* part = ex + 256;
  const int sub = 2 * wm + hh, pl = wn * 32 + r;
  ex[sub * 64 + pl] = (f32x2){e.re, e.im};
  bar_lds();
  cf es[4];
#pragma unroll
  for (int j = 0; j < 4; ++j) { const f32x2 t = ex[j * 64 + pl]; es[j] = {t[0], t[1]}; }
  gu64* gran = (gu64*)(ws + WS_GRAN);
  if (sub == 0) {
    cf E = es[0];
#pragma unroll
    for (int j = 1; j < 4; ++j) E = cfma(l32, E, es[j]);
    gu64* g = gran + ((size_t)c * 640 + p) * 2;
    __hip_atomic_store(g, (1ull << 32) | __float_as_uint(E.re), RLX_AGENT);
    __hip_atomic_store(g + 1, (1ull << 32) | __float_as_uint(E.im), RLX_AGENT);
  }
  STAMP(4);
  {
    const int pp = pt * 64 + lane;
    const cf L1 = {lb128[0], lb128[1]}, L2 = cmul(L1, L1), L4 = cmul(L2, L2);
    cf a = {0.f, 0.f};
    int last = -1;
    for (int cc = wave; cc < c; cc += 4) {
      gu64* g = gran + ((size_t)cc * 640 + pp) * 2;
      unsigned long long x0, x1;
      for (;;) {
        x0 = __hip_atomic_load(g, RLX_AGENT);
        x1 = __hip_atomic_load(g + 1, RLX_AGENT);
        if (__all((x0 >> 32) == 1ull && (x1 >> 32) == 1ull)) break;
        __builtin_amdgcn_s_sleep(2);
      }
      a = cfma(L4, a, (cf){__uint_as_float((unsigned)x0), __uint_as_float((unsigned)x1)});
      last = cc;
    }
    const int ex3 = c - 1 - last;
    if (last >= 0) {
      if (ex3 & 1) a = cmul(a, L1);
      if (ex3 & 2) a = cmul(a, L2);
    }
    part[wave * 64 + lane] = (f32x2){a.re, a.im};
  }
  bar_lds();
  STAMP(5);
  cf h;
  {
    cf hin = {0.f, 0.f};
#pragma unroll
    for (int j = 0; j < 4; ++j) { const f32x2 t = part[j * 64 + pl]; hin.re += t[0]; hin.im += t[1]; }
    h = hin;
#pragma unroll
    for (int j = 0; j < 3; ++j)
      if (j < sub) h = cfma(l32, h, es[j]);
  }
  bar_lds();
  constexpr int IP = 288;
#pragma unroll
  for (int mt = 0; mt < 2; ++mt) {
    unsigned wr[8], wi[8];
#pragma unroll
    for (int j = 0; j < 16; j += 2) {
      h = cfma(lam, h, (cf){acc[mt][0][j], acc[mt][1][j]});
      const cf h0 = h;
      h = cfma(lam, h, (cf){acc[mt][0][j + 1], acc[mt][1][j + 1]});
      wr[j >> 1] = pack2(h0.re, h.re);
      wi[j >> 1] = pack2(-h0.im, -h.im);
    }
    ldsp d = smem + pl * IP + (sub * 32 + mt * 16) * 2;
    *(LAS u32x4*)(d) = (u32x4){wr[0], wr[1], wr[2], wr[3]};
    *(LAS u32x4*)(d + 16) = (u32x4){wr[4], wr[5], wr[6], wr[7]};
    *(LAS u32x4*)(d + 64 * IP) = (u32x4){wi[0], wi[1], wi[2], wi[3]};
    *(LAS u32x4*)(d + 64 * IP + 16) = (u32x4){wi[4], wi[5], wi[6], wi[7]};
  }
  bar_lds();
  {
    const int g = lane >> 4, q = (lane & 15) >> 2, pq = lane & 3, i16 = lane & 15;
    u16* hb = (u16*)(ws + WS_H);
#pragma unroll
    for (int it = 0; it < 8; ++it) {
      const int item = it * 4 + wave, tb = item & 7, pb = item >> 3;
      ldsp a = smem + (32 * pb + 8 * g + q) * IP + (16 * tb + 4 * pq) * 2;
      const s16x4 lo = ldstr(a), hi = ldstr(a + 4 * IP);
      const s16x8 v = {lo[0], lo[1], lo[2], lo[3], hi[0], hi[1], hi[2], hi[3]};
      __builtin_amdgcn_raw_buffer_store_b128(__builtin_bit_cast(u32x4, v),
                                             __builtin_amdgcn_make_buffer_rsrc((void*)hb, 0, SEQ * 2 * DM * 2, 0x00020000),
                                             ((c * 128 + 16 * tb + i16) * (2 * DM) + (pb >> 1) * DM + pt * 64 + (pb & 1) * 32 + 8 * g) * 2, 0, H_AUX);
    }
    __builtin_amdgcn_s_setprio(0);
    asm volatile("s_waitcnt vmcnt(0)" ::: "memory");
    const unsigned sc = __builtin_amdgcn_readfirstlane((unsigned)(uintptr_t)smem + 47 * 1024);
#pragma unroll 1
    for (int i = 0; i < 8; ++i) {
      const int row = (wave * 4 + (i >> 1)) * 8 + (lane >> 3);
      glds16(ws + WS_H, ((c * 128 + row) * (2 * DM) + (i & 1) * DM + pt * 64) * 2 + (lane & 7) * 16, sc);
    }
  }
}

constexpr int K2_LDS = 5 * 16384;
#define NS1 160
#define S1_PRIO_C 2
#define S1_PRIO_L 0
constexpr int N_ATT = 256, N_S1 = NS1;
__global__ __launch_bounds__(512, 4) void k2_attn_ssm(K2In in, char* __restrict__ ws) {
  extern __shared__ __attribute__((aligned(16))) char smem_[];
  ldsp smem = (ldsp)smem_;
  const int tid = threadIdx.x, lane = tid & 63, wave = __builtin_amdgcn_readfirstlane(tid >> 6);
  STAMP_DECL STAMP(0);
  const int b = blockIdx.x;
  if (b < N_ATT) attention_role(b, ws, smem, wave, lane STAMP_PASS);
  else {
    if (wave >= 4) __builtin_amdgcn_s_setprio(S1_PRIO_L); else __builtin_amdgcn_s_setprio(S1_PRIO_C);
#define S1_DELAY 24
    if (wave >= 4) __builtin_amdgcn_s_sleep(S1_DELAY);
    s1_role(b - N_ATT, in, ws, smem, tid, wave, lane STAMP_PASS);
  }
  STAMP_END(ws, 1);
}

template <int NS>
__device__ __forceinline__ void out_loader(ldsp smem, int lw, int lane, const char* obase, const char* wbase) {
  typedef GemmGeo<2, 2, NS, NS> G;
  constexpr int NK = 12, D = NS - 1, NA = 4, NB = 4;
  const unsigned lds0 = (unsigned)(uintptr_t)smem;
  auto issueA = [&](int stage) {
#pragma unroll
    for (int i = 0; i < NA; ++i) {
      const int row = (lw * NA + i) * 8 + (lane >> 3), ch = 8 * stage + ((lane & 7) ^ swz64(row));
      const int head = (ch * 43) >> 7;
      glds16(obase, head * (SEQ * OREC) + row * OREC + (ch - 3 * head) * 16, lds0 + (stage % NS) * G::ASZ + (lw * NA + i) * 1024);
    }
  };
  auto issueB = [&](int stage) {
#pragma unroll
    for (int i = 0; i < NB; ++i) {
      const int row = (lw * NB + i) * 4 + (lane >> 4), k = 64 * stage + row;
      const int head = (k * 2731) >> 16;
      const int wrow = 20 * head + min(k - 24 * head, 19);
      glds16(wbase, wrow * (DM * 2) + (((lane & 15) ^ ((row & 3) << 2)) << 4), lds0 + G::BOFF + (stage % NS) * G::BSZ + (lw * NB + i) * 1024);
    }
  };
#pragma unroll
  for (int s = 0; s < D; ++s) { issueA(s); issueB(s); }
  static_for<0, NK>([&](auto ic) {
    constexpr int kt = decltype(ic)::value;
    wait_vm_bar<pieces_after<D, D, NA, NB, NK>(kt)>();
    if constexpr (kt + D < NK) { issueB(kt + D); issueA(kt + D); }
  });
}

#define K3_YNS 5
#define K3_ONS 4
constexpr int K3_LDS = (K3_YNS * 24576 > K3_ONS * 32768) ? K3_YNS * 24576 : K3_ONS * 32768;
struct K3In { const u16 *x, *cre, *cim, *dskip, *wo; };
__global__ __launch_bounds__(512, 2) void k3_out(K3In in, const char* __restrict__ ws, float* __restrict__ out) {
  extern __shared__ __attribute__((aligned(16))) char smem_[];
  ldsp smem = (ldsp)smem_;
  const int tid = threadIdx.x, lane = tid & 63, wave = __builtin_amdgcn_readfirstlane(tid >> 6);
  const int wm = wave >> 1, wn = wave & 1, r = lane & 31, hh = lane >> 5;
  const int b = blockIdx.x, idx = b >> 3, rt = 2 * (b & 7) + idx / 15, jt = idx % 15;
  STAMP_DECL STAMP(0);
  if (jt < 10) {
    const int ct = jt;
    constexpr int CP = 64 * 4 + 16;
    const int ec = ct * 64 + (tid & 15) * 4, er = rt * 128 + (tid >> 4);
    u32x2 xv[4];
    u32x2 dv;
    if (wave >= 4) {
      const int lw = wave - 4;
      unsigned aoff[4], boff[2];
      nk_offsets<4>(aoff, lw, lane, [&](int row) { return (unsigned)(row * (2 * DM * 2)); });
      nk_offsets<2>(boff, lw, lane, [&](int row) { return (unsigned)(row * (DM * 2)); });
      const char* bre = (const char*)in.cre + (size_t)ct * 64 * (DM * 2);
      const char* bim = (const char*)in.cim + (size_t)ct * 64 * (DM * 2);
      gemm_loader<K3_YNS, K3_YNS, 2, 1, 20>(smem, lw, ws + WS_H + (size_t)rt * 128 * (2 * DM * 2), aoff,
                                  [&](int s) { return (s < 10 ? bre : bim - 10 * 128) + (size_t)s * 128; }, boff);
#pragma unroll
      for (int i = 0; i < 4; ++i) xv[i] = *(const u32x2*)(in.x + (size_t)(er + 32 * i) * DM + ec);
      dv = *(const u32x2*)(in.dskip + ec);
      bar_lds();
    } else {
#pragma unroll
      for (int i = 0; i < 4; ++i) xv[i] = *(const u32x2*)(in.x + (size_t)(er + 32 * i) * DM + ec);
      dv = *(const u32x2*)(in.dskip + ec);
      f32x16 acc[2][1];
      zero_acc(acc);
      gemm_consumer<K3_YNS, K3_YNS, false, 2, 1, true, 20>(acc, smem, wave, lane STAMP_PASS);
      bar_lds();
#pragma unroll
      for (int mt = 0; mt < 2; ++mt)
#pragma unroll
        for (int g = 0; g < 4; ++g)
          *(LAS f32x4*)(smem + (wm * 64 + mt * 32 + r) * CP + (wn * 32 + 8 * g + 4 * hh) * 4) =
              (f32x4){acc[mt][0][4 * g], acc[mt][0][4 * g + 1], acc[mt][0][4 * g + 2], acc[mt][0][4 * g + 3]};
    }
    bar_lds();
    const float d0 = bflo(dv[0]), d1 = bfhi(dv[0]), d2 = bflo(dv[1]), d3 = bfhi(dv[1]);
#pragma unroll
    for (int i = 0; i < 4; ++i) {
      f32x4 y = *(const LAS f32x4*)(smem + ((tid >> 4) + 32 * i) * CP + (tid & 15) * 16);
      y[0] += rbf(bflo(xv[i][0]) * d0);
      y[1] += rbf(bfhi(xv[i][0]) * d1);
      y[2] += rbf(bflo(xv[i][1]) * d2);
      y[3] += rbf(bfhi(xv[i][1]) * d3);
      ST_OUT(out + (size_t)(er + 32 * i) * (2 * DM) + ec, y);
    }
  } else {
    const int ct = jt - 10;
    constexpr int CP = 128 * 4 + 16;
    if (wave >= 4) {
      const int lw = wave - 4;
      out_loader<K3_ONS>(smem, lw, lane, ws + WS_O + (size_t)rt * 128 * OREC, (const char*)in.wo + ct * 256);
      bar_lds();
    } else {
      f32x16 acc[2][2];
      zero_acc(acc);
      gemm_consumer<K3_ONS, K3_ONS, true, 2, 2, true, 12>(acc, smem, wave, lane STAMP_PASS);
      bar_lds();
#pragma unroll
      for (int mt = 0; mt < 2; ++mt)
#pragma unroll
        for (int nt = 0; nt < 2; ++nt)
#pragma unroll
          for (int g = 0; g < 4; ++g)
            *(LAS f32x4*)(smem + (wm * 64 + mt * 32 + r) * CP + (wn * 64 + nt * 32 + 8 * g + 4 * hh) * 4) =
                (f32x4){rbf(acc[mt][nt][4 * g]), rbf(acc[mt][nt][4 * g + 1]), rbf(acc[mt][nt][4 * g + 2]), rbf(acc[mt][nt][4 * g + 3])};
    }
    bar_lds();
#pragma unroll
    for (int i = 0; i < 8; ++i) {
      const int row = (tid >> 5) + 16 * i;
      const f32x4 y = *(const LAS f32x4*)(smem + row * CP + (tid & 31) * 16);
      ST_OUT(out + (size_t)(rt * 128 + row) * (2 * DM) + DM + ct * 128 + (tid & 31) * 4, y);
    }
  }
  STAMP_END((char*)ws, 2);
}

extern "C" void kernel_launch(void* const* d_in, const int* in_sizes, int n_in, void* d_out, int out_size, void* d_ws,
                              size_t ws_size, hipStream_t stream) {
  const u16* A_x = (const u16*)d_in[0];
  const u16* lre = (const u16*)d_in[1];
  const u16* lim = (const u16*)d_in[2];
  const u16* bre = (const u16*)d_in[3];
  const u16* bim = (const u16*)d_in[4];
  const u16* cre = (const u16*)d_in[5];
  const u16* cim = (const u16*)d_in[6];
  const u16* logdt = (const u16*)d_in[7];
  const u16* dskip = (const u16*)d_in[8];
  const u16* B_x = (const u16*)d_in[9];
  const u16* wqkv = (const u16*)d_in[10];
  const u16* wo = (const u16*)d_in[11];
  char* ws = (char*)d_ws;
  (void)hipFuncSetAttribute((const void*)k1_qkv, hipFuncAttributeMaxDynamicSharedMemorySize, K1_LDS);
  (void)hipFuncSetAttribute((const void*)k2_attn_ssm, hipFuncAttributeMaxDynamicSharedMemorySize, K2_LDS);
  (void)hipFuncSetAttribute((const void*)k3_out, hipFuncAttributeMaxDynamicSharedMemorySize, K3_LDS);
#define ONLY 7
  K1In in1 = {B_x, wqkv, lre, lim, logdt, A_x, bre, bim};
  if (ONLY & 1) k1_qkv<<<256, 512, K1_LDS, stream>>>(in1, ws);
  K2In in2 = {A_x, bre, bim, cre, cim, wo};
  if (ONLY & 2) k2_attn_ssm<<<N_ATT + N_S1, 512, K2_LDS, stream>>>(in2, ws);
  K3In in3 = {A_x, cre, cim, dskip, wo};
  if (ONLY & 4) k3_out<<<240, 512, K3_LDS, stream>>>(in3, ws, (float*)d_out);
}
```
